```python
import torch

def _softmax(x, axis=-1):
    return torch.softmax(x, dim=axis)
def _gelu(x):
    return torch.nn.functional.gelu(x, approximate="tanh")
def _silu(x):
    return torch.nn.functional.silu(x)
def _sigmoid(x):
    return torch.sigmoid(x)
def _layernorm(x, axis=-1, eps=1e-5):
    m = x.mean(dim=axis, keepdim=True)
    v = x.var(dim=axis, keepdim=True, unbiased=False)
    return (x - m) / torch.sqrt(v + eps)
def _scatter_add(tgt, idx, src, axis=0):
    return tgt.clone().index_add_(axis, idx, src)
def _segment_sum(src, seg, n):
    out = torch.zeros((n,) + src.shape[1:], dtype=src.dtype)
    return out.index_add_(0, seg, src)
def _pad(x, pw):
    flat = tuple(v for pair in reversed(pw) for v in pair)
    return torch.nn.functional.pad(x, flat)

kernel_name = 'gated_mix__spectral_fnet__mha_block__prompt'

def setup_inputs():

    _w = (lambda a: (lambda t: t.to(torch.bfloat16) if t.is_floating_point() else t)(torch.as_tensor(a)))
    def _A_setup():
        B, S, D = 1, 2048, 1024
        ns = torch.arange(S)[:, None] * torch.arange(S)[None, :]
        nd = torch.arange(D)[:, None] * torch.arange(D)[None, :]
        Ws_re = torch.cos(-2.0 * torch.pi * ns / S).to(float)
        Ws_im = torch.sin(-2.0 * torch.pi * ns / S).to(float)
        Wd_re = torch.cos(-2.0 * torch.pi * nd / D).to(float)
        Wd_im = torch.sin(-2.0 * torch.pi * nd / D).to(float)
        return {
            "x": _w((lambda *s: torch.randn(*s, dtype=torch.bfloat16) * 0.05)(B, S, D)),
            "dft_seq_re": _w(Ws_re),
            "dft_seq_im": _w(Ws_im),
            "dft_hid_re": _w(Wd_re),
            "dft_hid_im": _w(Wd_im),
        }
    _A_in = _A_setup()

    def _B_setup():
        B, S, D, H = 1, 2048, 1024, 32
        return {
            "x": _w((lambda *s: torch.randn(*s, dtype=torch.bfloat16) * 0.05)(B, S, D)),
            "wqkv": _w((lambda *s: torch.randn(*s, dtype=torch.bfloat16) * 1.6)(D, 3 * D)),
            "wo": _w((lambda *s: torch.randn(*s, dtype=torch.bfloat16) * 0.2)(D, D)),
        }
    _B_in = _B_setup()

    return {
        "A_x": _A_in["x"],
        "A_dft_seq_re": _A_in["dft_seq_re"],
        "A_dft_seq_im": _A_in["dft_seq_im"],
        "A_dft_hid_re": _A_in["dft_hid_re"],
        "A_dft_hid_im": _A_in["dft_hid_im"],
        "B_x": _B_in["x"],
        "B_wqkv": _B_in["wqkv"],
        "B_wo": _B_in["wo"],
        "gate_w": _w((lambda *s: torch.randn(*s, dtype=torch.bfloat16) * 0.05)(1024) * 16.0),
    }

def reference(A_x, A_dft_seq_re, A_dft_seq_im, A_dft_hid_re, A_dft_hid_im, B_x, B_wqkv, B_wo, gate_w):
    def _A_ref(x, dft_seq_re, dft_seq_im, dft_hid_re, dft_hid_im):
        a = torch.einsum("bsd,ed->bse", x, dft_hid_re)
        b = torch.einsum("bsd,ed->bse", x, dft_hid_im)
        y = torch.einsum("ts,bse->bte", dft_seq_re, a) - torch.einsum("ts,bse->bte", dft_seq_im, b)
        return y

    def _B_ref(x, wqkv, wo):
        B, S, D = x.shape
        H = 32
        Dh = D // H
        qkv = (lambda x, *d: x.permute(*d))((x @ wqkv).reshape(B, S, 3, H, Dh), 2, 0, 3, 1, 4)
        q, k, v = qkv[0], qkv[1], qkv[2]
        scores = torch.einsum("bhqd,bhkd->bhqk", q, k) * (Dh ** -0.5)
        p = _softmax(scores, axis=-1)
        o = torch.einsum("bhqk,bhkd->bhqd", p, v)
        return (lambda x, *d: x.permute(*d))(o, 0, 2, 1, 3).reshape(B, S, D) @ wo

    x = A_x
    g = _sigmoid((x * gate_w).sum(-1, keepdims=True))
    a = _A_ref(x, A_dft_seq_re, A_dft_seq_im, A_dft_hid_re, A_dft_hid_im)
    b = _B_ref(x, B_wqkv, B_wo)
    return g * a + (1 - g) * b
```

```cpp
#include <hip/hip_runtime.h>
#include <hip/hip_bf16.h>
typedef unsigned short u16;
__device__ inline float bf2f(u16 b) { return __uint_as_float(((unsigned)b) << 16); }
__device__ inline u16 f2bf(float f) { unsigned u = __float_as_uint(f); u += 0x7FFFu + ((u >> 16) & 1u); return (u16)(u >> 16); }
template<bool BT, bool OUTBF> __global__ void ngemm(const u16* A, const u16* B, void* C, int M, int N, int K, int ldb) {
  __shared__ float As[32][33], Bs[32][33];
  int tx = threadIdx.x, ty = threadIdx.y; int row = blockIdx.y * 32 + ty, col = blockIdx.x * 32 + tx; float acc = 0;
  for (int k0 = 0; k0 < K; k0 += 32) {
    As[ty][tx] = bf2f(A[(size_t)row * K + k0 + tx]);
    if (BT) Bs[tx][ty] = bf2f(B[(size_t)(blockIdx.x * 32 + ty) * ldb + k0 + tx]); else Bs[ty][tx] = bf2f(B[(size_t)(k0 + ty) * ldb + col]);
    __syncthreads();
    for (int k = 0; k < 32; ++k) acc += As[ty][k] * Bs[k][tx];
    __syncthreads();
  }
  if (OUTBF) ((u16*)C)[(size_t)row * N + col] = f2bf(acc); else ((float*)C)[(size_t)row * N + col] = acc;
}
__global__ void nattn(const u16* qkv, u16* o) {
  int h = blockIdx.y, q = blockIdx.x, lane = threadIdx.x;
  __shared__ float p[2048]; __shared__ float qs[32];
  if (lane < 32) qs[lane] = bf2f(qkv[(size_t)q * 3072 + h * 32 + lane]);
  __syncthreads();
  float m = -1e30f;
  for (int k = lane; k < 2048; k += 64) { float s = 0; for (int d = 0; d < 32; ++d) s += qs[d] * bf2f(qkv[(size_t)k * 3072 + 1024 + h * 32 + d]); s *= 0.17677669529f; p[k] = s; m = fmaxf(m, s); }
  for (int o2 = 32; o2; o2 >>= 1) m = fmaxf(m, __shfl_xor(m, o2));
  float l = 0; for (int k = lane; k < 2048; k += 64) { float e = __expf(p[k] - m); p[k] = e; l += e; }
  for (int o2 = 32; o2; o2 >>= 1) l += __shfl_xor(l, o2);
  __syncthreads();
  int d = lane & 31, hf = lane >> 5; float acc = 0;
  for (int k = hf; k < 2048; k += 2) acc += p[k] * bf2f(qkv[(size_t)k * 3072 + 2048 + h * 32 + d]);
  acc += __shfl_xor(acc, 32);
  if (lane < 32) o[(size_t)q * 1024 + h * 32 + d] = f2bf(acc / l);
}
__global__ void ncomb(const u16* x, const u16* gw, const float* yr, const float* yi, const float* bb, u16* out) {
  int r = blockIdx.x, t = threadIdx.x; __shared__ float red[256]; float s = 0;
  for (int d = t; d < 1024; d += 256) s += bf2f(x[r * 1024 + d]) * bf2f(gw[d]);
  red[t] = s; __syncthreads(); for (int o = 128; o; o >>= 1) { if (t < o) red[t] += red[t + o]; __syncthreads(); }
  float g = 1.f / (1.f + __expf(-red[0]));
  for (int d = t; d < 1024; d += 256) out[r * 1024 + d] = f2bf(g * (yr[r * 1024 + d] - yi[r * 1024 + d]) + (1.f - g) * bb[r * 1024 + d]);
}
extern "C" void kernel_launch(void* const* d_in, const int* in_sizes, int n_in, void* d_out, int out_size, void* d_ws, size_t ws_size, hipStream_t stream) {
  const u16 *x = (const u16*)d_in[0], *cr = (const u16*)d_in[1], *ci = (const u16*)d_in[2], *dr = (const u16*)d_in[3], *di = (const u16*)d_in[4], *wqkv = (const u16*)d_in[6], *wo = (const u16*)d_in[7], *gw = (const u16*)d_in[8];
  char* ws = (char*)d_ws; u16* a = (u16*)ws; u16* b = (u16*)(ws + (4 << 20)); u16* qkv = (u16*)(ws + (8 << 20)); u16* o = (u16*)(ws + (20 << 20));
  float* yr = (float*)(ws + (24 << 20)); float* yi = (float*)(ws + (32 << 20)); float* bb = (float*)(ws + (40 << 20));
  dim3 bl(32, 32);
  ngemm<true, true><<<dim3(32, 64), bl, 0, stream>>>(x, dr, a, 2048, 1024, 1024, 1024);
  ngemm<true, true><<<dim3(32, 64), bl, 0, stream>>>(x, di, b, 2048, 1024, 1024, 1024);
  ngemm<false, true><<<dim3(96, 64), bl, 0, stream>>>(x, wqkv, qkv, 2048, 3072, 1024, 3072);
  ngemm<false, false><<<dim3(32, 64), bl, 0, stream>>>(cr, a, yr, 2048, 1024, 2048, 1024);
  ngemm<false, false><<<dim3(32, 64), bl, 0, stream>>>(ci, b, yi, 2048, 1024, 2048, 1024);
  nattn<<<dim3(2048, 32), 64, 0, stream>>>(qkv, o);
  ngemm<false, false><<<dim3(32, 64), bl, 0, stream>>>(o, wo, bb, 2048, 1024, 1024, 1024);
  ncomb<<<2048, 256, 0, stream>>>(x, gw, yr, yi, bb, (u16*)d_out);
}
```

```cpp
#include <hip/hip_runtime.h>
#include <hip/hip_bf16.h>

__global__ void __launch_bounds__(512, 2)
fused_kernel(const void* x, const void* cr, const void* ci, const void* dr, const void* di,
             const void* wqkv, const void* wo, const void* gw, void* out, void* ws) {
    __shared__ __attribute__((aligned(16))) char lds[163840];
    asm volatile(
"L_start_%=:\n\t"
"s_load_dwordx2 s[8:9], %0, 0x0\n\t"
"s_load_dwordx2 s[16:17], %0, 0x18\n\t"
"s_load_dwordx2 s[20:21], %0, 0x20\n\t"
"s_load_dwordx2 s[12:13], %0, 0x28\n\t"
"s_load_dwordx4 s[80:83], %0, 0x8\n\t"
"s_load_dwordx8 s[84:91], %0, 0x30\n\t"
"s_getpc_b64 s[96:97]\n\t"
"L_pc0_%=:\n\t"
"s_lshr_b32 s4, %1, 3\n\t"
"s_and_b32 s97, s97, 0xffff\n\t"
"s_lshl_b32 s4, s4, 11\n\t"
"s_mov_b32 s98, L_end_%=-L_pc0_%=\n\t"
"s_mov_b32 s99, 0x20000\n\t"
"v_lshlrev_b32 v1, 2, %2\n\t"
"buffer_load_dword v246, v1, s[96:99], s4 offen\n\t"
"s_mov_b32 s55, %1\n\t"
"v_and_b32 v252, 63, %2\n\t"
"v_lshrrev_b32 v0, 6, %2\n\t"
"s_and_b32 s5, %1, 7\n\t"
"v_readfirstlane_b32 s44, v0\n\t"
"v_and_b32 v253, 15, v252\n\t"
"v_lshrrev_b32 v254, 4, v252\n\t"
"s_lshr_b32 s4, %1, 3\n\t"
"s_and_b32 s46, s4, 7\n\t"
"s_lshr_b32 s61, s4, 3\n\t"
"s_lshl_b32 s47, s5, 2\n\t"
"s_add_u32 s47, s47, s61\n\t"
"s_and_b32 s45, s44, 3\n\t"
"s_mov_b32 s10, 0x400000\n\t"
"s_mov_b32 s11, 0x20000\n\t"
"s_mov_b32 s18, 0x200000\n\t"
"s_mov_b32 s19, 0x20000\n\t"
"s_mov_b32 s22, 0x200400\n\t"
"s_mov_b32 s23, 0x20000\n\t"
"s_mov_b32 s14, 0x600000\n\t"
"s_mov_b32 s15, 0x20000\n\t"
"s_cmp_lt_u32 s44, 4\n\t"
"s_cbranch_scc1 L_compute_%=\n\t"
"v_lshrrev_b32 v16, 3, v252\n\t"
"v_and_b32 v17, 7, v252\n\t"
"v_lshrrev_b32 v18, 2, v16\n\t"
"v_lshlrev_b32 v18, 3, v18\n\t"
"v_and_b32 v19, 3, v16\n\t"
"v_add_u32 v18, v18, v19\n\t"
"v_lshlrev_b32 v18, 11, v18\n\t"
"v_lshrrev_b32 v19, 1, v16\n\t"
"v_xor_b32 v19, v19, v17\n\t"
"v_lshl_add_u32 v18, v19, 4, v18\n\t"
"s_lshl_b32 s59, s46, 19\n\t"
"s_lshl_b32 s60, s45, 17\n\t"
"s_add_u32 s59, s59, s60\n\t"
"v_add_u32 v0, s59, v18\n\t"
"v_xor_b32 v20, 64, v0\n\t"
"v_add_u32 v1, 31744, v20\n\t"
"v_add_u32 v2, 6144, v0\n\t"
"v_add_u32 v3, 37888, v20\n\t"
"v_add_u32 v4, 65536, v0\n\t"
"v_add_u32 v5, 97280, v20\n\t"
"v_add_u32 v6, 71680, v0\n\t"
"v_add_u32 v7, 103424, v20\n\t"
"s_lshl_b32 s62, s45, 13\n\t"
"s_mov_b32 s65, 0\n\t"
"s_mov_b32 s58, 0\n\t"
"s_waitcnt lgkmcnt(0)\n\t"
"s_sub_u32 s20, s20, 1024\n\t"
"s_subb_u32 s21, s21, 0\n\t"
"s_and_b32 s9, s9, 0xffff\n\t"
"s_and_b32 s17, s17, 0xffff\n\t"
"s_and_b32 s21, s21, 0xffff\n\t"
"s_and_b32 s13, s13, 0xffff\n\t"
"s_add_u32 m0, s58, s62\n\t"
"s_nop 0\n\t"
"buffer_load_dwordx4 v0, s[8:11], s65 offen lds\n\t"
"buffer_load_dwordx4 v1, s[8:11], s65 offen offset:1024 lds\n\t"
"buffer_load_dwordx4 v2, s[8:11], s65 offen offset:2048 lds\n\t"
"buffer_load_dwordx4 v3, s[8:11], s65 offen offset:3072 lds\n\t"
"s_add_u32 m0, m0, 4096\n\t"
"s_nop 0\n\t"
"buffer_load_dwordx4 v4, s[8:11], s65 offen lds\n\t"
"buffer_load_dwordx4 v5, s[8:11], s65 offen offset:1024 lds\n\t"
"buffer_load_dwordx4 v6, s[8:11], s65 offen offset:2048 lds\n\t"
"buffer_load_dwordx4 v7, s[8:11], s65 offen offset:3072 lds\n\t"
"s_lshl_b32 s59, s47, 5\n\t"
"s_lshl_b32 s60, s45, 3\n\t"
"s_add_u32 s59, s59, s60\n\t"
"v_add_u32 v18, s59, v16\n\t"
"v_lshlrev_b32 v18, 11, v18\n\t"
"s_and_b32 s60, s45, 1\n\t"
"s_lshl_b32 s60, s60, 6\n\t"
"v_lshl_add_u32 v11, v19, 4, v18\n\t"
"v_xor_b32 v11, s60, v11\n\t"
"v_lshrrev_b32 v18, 2, v252\n\t"
"v_and_b32 v19, 3, v252\n\t"
"v_lshrrev_b32 v20, 5, v252\n\t"
"v_lshlrev_b32 v20, 1, v20\n\t"
"v_xor_b32 v19, v19, v20\n\t"
"s_lshl_b32 s59, s45, 4\n\t"
"v_add_u32 v18, s59, v18\n\t"
"v_mul_u32_u24 v18, 6144, v18\n\t"
"v_lshl_add_u32 v8, v19, 4, v18\n\t"
"s_lshl_b32 s59, s47, 6\n\t"
"v_add_u32 v8, s59, v8\n\t"
"v_add_u32 v9, 1024, v8\n\t"
"v_add_u32 v10, 2048, v8\n\t"
"s_mul_i32 s63, s45, 3072\n\t"
"s_add_u32 s63, s63, 32768\n\t"
"s_lshl_b32 s64, s45, 11\n\t"
"s_add_u32 s64, s64, 45056\n\t"
"s_mov_b32 s66, 0\n\t"
"s_add_u32 m0, s58, s63\n\t"
"s_nop 0\n\t"
"buffer_load_dwordx4 v8, s[12:15], s66 offen lds\n\t"
"buffer_load_dwordx4 v9, s[12:15], s66 offen offset:1024 lds\n\t"
"buffer_load_dwordx4 v10, s[12:15], s66 offen offset:2048 lds\n\t"
"s_add_u32 m0, s58, s64\n\t"
"s_nop 0\n\t"
"buffer_load_dwordx4 v11, s[16:19], s65 offen lds\n\t"
"buffer_load_dwordx4 v11, s[20:23], s65 offen offset:1024 lds\n\t"
"s_add_u32 s65, s65, 128\n\t"
"s_add_u32 s66, s66, 393216\n\t"
"s_add_u32 s58, s58, 53248\n\t"
"s_cmp_eq_u32 s58, 159744\n\t"
"s_cselect_b32 s58, 0, s58\n\t"
"s_cmp_eq_u32 s45, 0\n\t"
"s_cselect_b32 s60, 7, 3\n\t"
"v_lshrrev_b32 v16, 2, v252\n\t"
"v_and_b32 v16, s60, v16\n\t"
"v_and_b32 v17, 3, v252\n\t"
"v_lshlrev_b32 v17, 7, v17\n\t"
"v_lshl_add_u32 v28, v16, 11, v17\n\t"
"s_lshl_b32 s59, s5, 5\n\t"
"s_add_u32 s59, s59, s4\n\t"
"s_lshl_b32 s59, s59, 13\n\t"
"s_lshl_b32 s60, s55, 14\n\t"
"s_cmp_eq_u32 s45, 0\n\t"
"s_cselect_b32 s59, s60, s59\n\t"
"s_cmp_eq_u32 s45, 2\n\t"
"s_cselect_b32 s60, 1024, 0\n\t"
"s_add_u32 s59, s59, s60\n\t"
"v_add_u32 v28, s59, v28\n\t"
"s_mov_b32 s67, 512\n\t"
"s_cmp_lg_u32 s45, 3\n\t"
"s_cbranch_scc1 L_cla103_%=\n\t"
"v_bfe_u32 v16, v252, 1, 2\n\t"
"v_bfe_u32 v17, v252, 3, 1\n\t"
"v_lshl_add_u32 v16, v17, 7, v16\n\t"
"s_lshl_b32 s59, s4, 2\n\t"
"v_add_u32 v16, s59, v16\n\t"
"v_mul_u32_u24 v16, 6144, v16\n\t"
"v_min_u32 v17, 2, v254\n\t"
"v_lshl_add_u32 v16, v17, 11, v16\n\t"
"v_and_b32 v17, 1, v252\n\t"
"v_lshl_add_u32 v16, v17, 7, v16\n\t"
"s_lshl_b32 s59, s5, 8\n\t"
"v_add_u32 v28, s59, v16\n\t"
"s_mov_b32 s67, 1572864\n\t"
"L_cla103_%=:\n\t"
"s_mov_b64 s[72:73], s[8:9]\n\t"
"s_mov_b32 s74, s10\n\t"
"s_mov_b32 s75, 0x20000\n\t"
"s_cmp_eq_u32 s45, 1\n\t"
"s_cselect_b64 s[72:73], s[16:17], s[72:73]\n\t"
"s_cselect_b32 s74, s18, s74\n\t"
"s_cmp_eq_u32 s45, 2\n\t"
"s_cselect_b64 s[72:73], s[20:21], s[72:73]\n\t"
"s_cselect_b32 s74, s22, s74\n\t"
"s_cmp_eq_u32 s45, 3\n\t"
"s_cselect_b64 s[72:73], s[12:13], s[72:73]\n\t"
"s_cselect_b32 s74, s14, s74\n\t"
"s_add_u32 m0, s58, s62\n\t"
"s_nop 0\n\t"
"buffer_load_dwordx4 v0, s[8:11], s65 offen lds\n\t"
"buffer_load_dwordx4 v1, s[8:11], s65 offen offset:1024 lds\n\t"
"buffer_load_dwordx4 v2, s[8:11], s65 offen offset:2048 lds\n\t"
"buffer_load_dwordx4 v3, s[8:11], s65 offen offset:3072 lds\n\t"
"s_add_u32 m0, m0, 4096\n\t"
"s_nop 0\n\t"
"buffer_load_dwordx4 v4, s[8:11], s65 offen lds\n\t"
"buffer_load_dwordx4 v5, s[8:11], s65 offen offset:1024 lds\n\t"
"buffer_load_dwordx4 v6, s[8:11], s65 offen offset:2048 lds\n\t"
"buffer_load_dwordx4 v7, s[8:11], s65 offen offset:3072 lds\n\t"
"s_waitcnt vmcnt(8)\n\t"
"s_barrier\n\t"
"s_add_u32 m0, s58, s63\n\t"
"s_nop 0\n\t"
"buffer_load_dwordx4 v8, s[12:15], s66 offen lds\n\t"
"buffer_load_dwordx4 v9, s[12:15], s66 offen offset:1024 lds\n\t"
"buffer_load_dwordx4 v10, s[12:15], s66 offen offset:2048 lds\n\t"
"s_add_u32 m0, s58, s64\n\t"
"s_nop 0\n\t"
"buffer_load_dwordx4 v11, s[16:19], s65 offen lds\n\t"
"buffer_load_dwordx4 v11, s[20:23], s65 offen offset:1024 lds\n\t"
"s_add_u32 s65, s65, 128\n\t"
"s_add_u32 s66, s66, 393216\n\t"
"s_add_u32 s58, s58, 53248\n\t"
"s_cmp_eq_u32 s58, 159744\n\t"
"s_cselect_b32 s58, 0, s58\n\t"
"s_add_u32 m0, s58, s62\n\t"
"s_nop 0\n\t"
"buffer_load_dwordx4 v0, s[8:11], s65 offen lds\n\t"
"buffer_load_dwordx4 v1, s[8:11], s65 offen offset:1024 lds\n\t"
"buffer_load_dwordx4 v2, s[8:11], s65 offen offset:2048 lds\n\t"
"buffer_load_dwordx4 v3, s[8:11], s65 offen offset:3072 lds\n\t"
"s_add_u32 m0, m0, 4096\n\t"
"s_nop 0\n\t"
"buffer_load_dwordx4 v4, s[8:11], s65 offen lds\n\t"
"buffer_load_dwordx4 v5, s[8:11], s65 offen offset:1024 lds\n\t"
"buffer_load_dwordx4 v6, s[8:11], s65 offen offset:2048 lds\n\t"
"buffer_load_dwordx4 v7, s[8:11], s65 offen offset:3072 lds\n\t"
"s_add_u32 m0, s58, s63\n\t"
"s_nop 0\n\t"
"buffer_load_dwordx4 v8, s[12:15], s66 offen lds\n\t"
"buffer_load_dwordx4 v9, s[12:15], s66 offen offset:1024 lds\n\t"
"buffer_load_dwordx4 v10, s[12:15], s66 offen offset:2048 lds\n\t"
"s_add_u32 m0, s58, s64\n\t"
"s_nop 0\n\t"
"buffer_load_dwordx4 v11, s[16:19], s65 offen lds\n\t"
"buffer_load_dwordx4 v11, s[20:23], s65 offen offset:1024 lds\n\t"
"s_add_u32 s65, s65, 128\n\t"
"s_add_u32 s66, s66, 393216\n\t"
"s_add_u32 s58, s58, 53248\n\t"
"s_cmp_eq_u32 s58, 159744\n\t"
"s_cselect_b32 s58, 0, s58\n\t"
"s_cmp_eq_u32 s45, 0\n\t"
"s_cselect_b32 s92, 0x33333333, 0\n\t"
"s_mov_b32 s93, 0\n\t"
"s_cmp_eq_u32 s45, 3\n\t"
"s_cselect_b32 s92, 0x00ff00ff, s92\n\t"
"s_cselect_b32 s93, 0xff, 0\n\t"
"s_cmp_eq_u32 s92, 0\n\t"
"s_cselect_b32 s92, 0x3333, s92\n\t"
"s_cmp_eq_u32 s45, 3\n\t"
"s_cselect_b32 s59, 8, 2\n\t"
"s_lshl_b64 s[94:95], s[92:93], s59\n\t"
"s_mov_b64 exec, s[92:93]\n\t"
"buffer_load_dword v64, v28, s[72:75], s67 offen\n\t"
"s_mov_b64 exec, -1\n\t"
"s_mov_b32 s79, 3\n\t"
"s_mov_b32 s78, 12\n\t"
"s_lshr_b32 s48, s5, 1\n\t"
"s_and_b32 s49, s5, 1\n\t"
"s_lshl_b32 s49, s49, 2\n\t"
"s_and_b32 s61, s4, 3\n\t"
"s_add_u32 s49, s49, s61\n\t"
"s_lshr_b32 s50, s4, 2\n\t"
"s_and_b32 s51, s48, 1\n\t"
"s_lshr_b32 s52, s48, 1\n\t"
"s_cmp_lt_u32 s48, 2\n\t"
"s_cselect_b32 s28, s80, s82\n\t"
"s_cselect_b32 s29, s81, s83\n\t"
"s_mov_b32 s28, s28\n\t"
"s_and_b32 s29, s29, 0xffff\n\t"
"s_mov_b32 s30, 0x800000\n\t"
"s_mov_b32 s31, 0x20000\n\t"
"s_mov_b32 s32, s84\n\t"
"s_and_b32 s33, s85, 0xffff\n\t"
"s_mov_b32 s34, 0x200000\n\t"
"s_mov_b32 s35, 0x20000\n\t"
"s_mov_b32 s40, s86\n\t"
"s_and_b32 s41, s87, 0xffff\n\t"
"s_mov_b32 s42, 2048\n\t"
"s_mov_b32 s43, 0x20000\n\t"
"s_mov_b32 s36, s88\n\t"
"s_and_b32 s37, s89, 0xffff\n\t"
"s_mov_b32 s38, 0x400000\n\t"
"s_mov_b32 s39, 0x20000\n\t"
"s_mov_b32 s24, s90\n\t"
"s_and_b32 s25, s91, 0xffff\n\t"
"s_mov_b32 s26, 0x4000000\n\t"
"s_mov_b32 s27, 0x20000\n\t"
"s_add_u32 s90, s90, 33583104\n\t"
"s_addc_u32 s91, s91, 0\n\t"
"s_load_dword s54, s[90:91], 0x0\n\t"
"s_waitcnt vmcnt(14)\n\t"
"s_mov_b32 s56, 13\n\t"
"s_branch L_g1l0_%=\n\t"
"L_g1l_%=:\n\t"
"s_waitcnt vmcnt(15)\n\t"
"L_g1l0_%=:\n\t"
"s_barrier\n\t"
"s_add_u32 m0, s58, s62\n\t"
"s_nop 0\n\t"
"buffer_load_dwordx4 v0, s[8:11], s65 offen lds\n\t"
"buffer_load_dwordx4 v1, s[8:11], s65 offen offset:1024 lds\n\t"
"buffer_load_dwordx4 v2, s[8:11], s65 offen offset:2048 lds\n\t"
"buffer_load_dwordx4 v3, s[8:11], s65 offen offset:3072 lds\n\t"
"s_add_u32 m0, m0, 4096\n\t"
"s_nop 0\n\t"
"buffer_load_dwordx4 v4, s[8:11], s65 offen lds\n\t"
"buffer_load_dwordx4 v5, s[8:11], s65 offen offset:1024 lds\n\t"
"buffer_load_dwordx4 v6, s[8:11], s65 offen offset:2048 lds\n\t"
"buffer_load_dwordx4 v7, s[8:11], s65 offen offset:3072 lds\n\t"
"s_add_u32 m0, s58, s63\n\t"
"s_nop 0\n\t"
"buffer_load_dwordx4 v8, s[12:15], s66 offen lds\n\t"
"buffer_load_dwordx4 v9, s[12:15], s66 offen offset:1024 lds\n\t"
"buffer_load_dwordx4 v10, s[12:15], s66 offen offset:2048 lds\n\t"
"s_add_u32 m0, s58, s64\n\t"
"s_nop 0\n\t"
"buffer_load_dwordx4 v11, s[16:19], s65 offen lds\n\t"
"buffer_load_dwordx4 v11, s[20:23], s65 offen offset:1024 lds\n\t"
"s_add_u32 s65, s65, 128\n\t"
"s_add_u32 s66, s66, 393216\n\t"
"s_add_u32 s58, s58, 53248\n\t"
"s_cmp_eq_u32 s58, 159744\n\t"
"s_cselect_b32 s58, 0, s58\n\t"
"s_bitcmp1_b32 s79, 0\n\t"
"s_cselect_b64 exec, s[94:95], s[92:93]\n\t"
"s_lshr_b32 s59, s79, 1\n\t"
"s_cmp_eq_u32 s56, s78\n\t"
"s_cselect_b64 exec, exec, 1\n\t"
"s_cselect_b32 s59, s59, 0\n\t"
"s_cselect_b32 s60, 1, 0\n\t"
"s_add_u32 s79, s79, s60\n\t"
"s_mul_i32 s60, s60, 2\n\t"
"s_sub_u32 s78, s78, s60\n\t"
"s_cmp_eq_u32 s79, 8\n\t"
"s_cselect_b32 s78, 0, s78\n\t"
"s_mul_i32 s59, s59, s67\n\t"
"buffer_load_dword v64, v28, s[72:75], s59 offen\n\t"
"s_mov_b64 exec, -1\n\t"
"s_sub_u32 s56, s56, 1\n\t"
"s_cmp_lg_u32 s56, 0\n\t"
"s_cbranch_scc1 L_g1l_%=\n\t"
"s_waitcnt vmcnt(15)\n\t"
"s_barrier\n\t"
"s_cmp_lg_u32 s45, 0\n\t"
"s_cbranch_scc1 L_cpa_%=\n\t"
"s_getpc_b64 s[96:97]\n\t"
"L_pca_%=:\n\t"
"s_sub_u32 s96, s96, L_pca_%=-L_start_%=\n\t"
"s_subb_u32 s97, s97, 0\n\t"
"s_and_b32 s97, s97, 0xffff\n\t"
"s_mov_b32 s98, L_end_%=-L_start_%=\n\t"
"s_mov_b32 s99, 0x20000\n\t"
"s_lshl_b32 s59, s4, 11\n\t"
"v_lshlrev_b32 v21, 7, v253\n\t"
"buffer_load_dword v21, v21, s[96:99], s59 offen\n\t"
"L_cpa_%=:\n\t"
"v_and_b32 v16, 31, v252\n\t"
"v_lshlrev_b32 v16, 4, v16\n\t"
"v_lshrrev_b32 v17, 5, v252\n\t"
"s_lshl_b32 s59, s46, 8\n\t"
"s_lshl_b32 s60, s47, 3\n\t"
"s_add_u32 s59, s59, s60\n\t"
"s_lshl_b32 s60, s45, 1\n\t"
"s_add_u32 s59, s59, s60\n\t"
"v_add_u32 v17, s59, v17\n\t"
"v_lshl_add_u32 v18, v17, 11, v16\n\t"
"buffer_load_dwordx4 v[32:35], v18, s[8:11], 0 offen offset:0\n\t"
"buffer_load_dwordx4 v[36:39], v18, s[8:11], 0 offen offset:512\n\t"
"buffer_load_dwordx4 v[40:43], v18, s[8:11], 0 offen offset:1024\n\t"
"buffer_load_dwordx4 v[44:47], v18, s[8:11], 0 offen offset:1536\n\t"
"buffer_load_dwordx4 v[48:51], v16, s[40:43], 0 offen offset:0\n\t"
"buffer_load_dwordx4 v[52:55], v16, s[40:43], 0 offen offset:512\n\t"
"buffer_load_dwordx4 v[56:59], v16, s[40:43], 0 offen offset:1024\n\t"
"buffer_load_dwordx4 v[60:63], v16, s[40:43], 0 offen offset:1536\n\t"
"s_mov_b32 s16, s80\n\t"
"s_and_b32 s17, s81, 0xffff\n\t"
"s_mov_b32 s18, 0x800000\n\t"
"s_mov_b32 s19, 0x20000\n\t"
"s_mov_b32 s20, s82\n\t"
"s_and_b32 s21, s83, 0xffff\n\t"
"s_mov_b32 s22, 0x800000\n\t"
"s_mov_b32 s23, 0x20000\n\t"
"s_lshl_b32 s59, s49, 1\n\t"
"s_add_u32 s59, s59, s51\n\t"
"s_lshl_b32 s59, s59, 2\n\t"
"s_add_u32 s59, s59, s45\n\t"
"s_lshl_b32 s59, s59, 17\n\t"
"v_lshrrev_b32 v248, 3, v252\n\t"
"v_and_b32 v249, 7, v252\n\t"
"v_lshrrev_b32 v244, 1, v248\n\t"
"v_xor_b32 v249, v249, v244\n\t"
"v_lshlrev_b32 v248, 12, v248\n\t"
"v_lshl_add_u32 v248, v249, 4, v248\n\t"
"v_add_u32 v244, s59, v248\n\t"
"v_xor_b32 v245, 64, v244\n\t"
"v_add_u32 v245, 31744, v245\n\t"
"v_add_u32 v246, 63488, v244\n\t"
"v_add_u32 v247, 63488, v245\n\t"
"v_lshrrev_b32 v248, 1, v253\n\t"
"v_xor_b32 v248, v248, v254\n\t"
"v_lshlrev_b32 v248, 4, v248\n\t"
"v_lshl_add_u32 v248, v253, 7, v248\n\t"
"s_lshl_b32 s63, s45, 12\n\t"
"s_add_u32 s59, s63, 131072\n\t"
"v_add_u32 v248, s59, v248\n\t"
"v_xor_b32 v249, 64, v248\n\t"
"s_lshl_b32 s59, s49, 1\n\t"
"s_add_u32 s59, s59, s51\n\t"
"s_lshl_b32 s59, s59, 3\n\t"
"s_add_u32 s59, s59, s50\n\t"
"s_lshl_b32 s59, s59, 2\n\t"
"s_add_u32 s59, s59, s45\n\t"
"s_lshl_b32 s59, s59, 14\n\t"
"v_lshrrev_b32 v15, 3, v252\n\t"
"v_lshlrev_b32 v15, 12, v15\n\t"
"v_bfe_u32 v25, v252, 2, 1\n\t"
"v_lshl_add_u32 v15, v25, 11, v15\n\t"
"v_and_b32 v25, 3, v252\n\t"
"v_lshl_add_u32 v15, v25, 7, v15\n\t"
"v_add_u32 v15, s59, v15\n\t"
"s_lshl_b32 s59, s49, 8\n\t"
"s_lshl_b32 s60, s50, 5\n\t"
"s_add_u32 s59, s59, s60\n\t"
"s_lshl_b32 s60, s45, 3\n\t"
"s_add_u32 s59, s59, s60\n\t"
"s_lshl_b32 s59, s59, 12\n\t"
"s_mul_i32 s60, s51, 2048\n\t"
"s_add_u32 s59, s59, s60\n\t"
"v_lshrrev_b32 v14, 2, v252\n\t"
"v_lshlrev_b32 v14, 12, v14\n\t"
"v_lshl_add_u32 v14, v25, 7, v14\n\t"
"v_add_u32 v14, s59, v14\n\t"
"v_lshrrev_b32 v250, 2, v252\n\t"
"v_lshlrev_b32 v250, 11, v250\n\t"
"v_and_b32 v25, 1, v254\n\t"
"v_lshlrev_b32 v25, 1, v25\n\t"
"v_and_b32 v26, 3, v252\n\t"
"v_xor_b32 v25, v25, v26\n\t"
"v_lshl_add_u32 v250, v25, 4, v250\n\t"
"v_lshrrev_b32 v25, 1, v254\n\t"
"v_and_b32 v26, 1, v254\n\t"
"v_lshrrev_b32 v27, 2, v253\n\t"
"v_lshl_add_u32 v27, v26, 2, v27\n\t"
"v_lshl_add_u32 v27, v25, 4, v27\n\t"
"v_lshlrev_b32 v27, 6, v27\n\t"
"v_bfe_u32 v25, v253, 1, 1\n\t"
"v_lshl_add_u32 v25, v26, 1, v25\n\t"
"v_lshl_add_u32 v27, v25, 4, v27\n\t"
"v_and_b32 v25, 1, v253\n\t"
"v_lshl_add_u32 v27, v25, 3, v27\n\t"
"s_lshl_b32 s59, s45, 12\n\t"
"s_add_u32 s59, s59, 147456\n\t"
"v_add_u32 v251, s59, v27\n\t"
"v_xor_b32 v10, 32, v251\n\t"
"s_waitcnt vmcnt(0)\n\t"
"s_barrier\n\t"
"v_mov_b32 v19, 0\n\t"
"v_mov_b32 v20, 0\n\t"
"v_lshlrev_b32 v21, 16, v32\n\t"
"v_lshlrev_b32 v22, 16, v48\n\t"
"v_and_b32 v23, 0xffff0000, v32\n\t"
"v_and_b32 v24, 0xffff0000, v48\n\t"
"v_fmac_f32 v19, v21, v22\n\t"
"v_fmac_f32 v20, v23, v24\n\t"
"v_lshlrev_b32 v21, 16, v33\n\t"
"v_lshlrev_b32 v22, 16, v49\n\t"
"v_and_b32 v23, 0xffff0000, v33\n\t"
"v_and_b32 v24, 0xffff0000, v49\n\t"
"v_fmac_f32 v19, v21, v22\n\t"
"v_fmac_f32 v20, v23, v24\n\t"
"v_lshlrev_b32 v21, 16, v34\n\t"
"v_lshlrev_b32 v22, 16, v50\n\t"
"v_and_b32 v23, 0xffff0000, v34\n\t"
"v_and_b32 v24, 0xffff0000, v50\n\t"
"v_fmac_f32 v19, v21, v22\n\t"
"v_fmac_f32 v20, v23, v24\n\t"
"v_lshlrev_b32 v21, 16, v35\n\t"
"v_lshlrev_b32 v22, 16, v51\n\t"
"v_and_b32 v23, 0xffff0000, v35\n\t"
"v_and_b32 v24, 0xffff0000, v51\n\t"
"v_fmac_f32 v19, v21, v22\n\t"
"v_fmac_f32 v20, v23, v24\n\t"
"v_lshlrev_b32 v21, 16, v36\n\t"
"v_lshlrev_b32 v22, 16, v52\n\t"
"v_and_b32 v23, 0xffff0000, v36\n\t"
"v_and_b32 v24, 0xffff0000, v52\n\t"
"v_fmac_f32 v19, v21, v22\n\t"
"v_fmac_f32 v20, v23, v24\n\t"
"v_lshlrev_b32 v21, 16, v37\n\t"
"v_lshlrev_b32 v22, 16, v53\n\t"
"v_and_b32 v23, 0xffff0000, v37\n\t"
"v_and_b32 v24, 0xffff0000, v53\n\t"
"v_fmac_f32 v19, v21, v22\n\t"
"v_fmac_f32 v20, v23, v24\n\t"
"v_lshlrev_b32 v21, 16, v38\n\t"
"v_lshlrev_b32 v22, 16, v54\n\t"
"v_and_b32 v23, 0xffff0000, v38\n\t"
"v_and_b32 v24, 0xffff0000, v54\n\t"
"v_fmac_f32 v19, v21, v22\n\t"
"v_fmac_f32 v20, v23, v24\n\t"
"v_lshlrev_b32 v21, 16, v39\n\t"
"v_lshlrev_b32 v22, 16, v55\n\t"
"v_and_b32 v23, 0xffff0000, v39\n\t"
"v_and_b32 v24, 0xffff0000, v55\n\t"
"v_fmac_f32 v19, v21, v22\n\t"
"v_fmac_f32 v20, v23, v24\n\t"
"v_lshlrev_b32 v21, 16, v40\n\t"
"v_lshlrev_b32 v22, 16, v56\n\t"
"v_and_b32 v23, 0xffff0000, v40\n\t"
"v_and_b32 v24, 0xffff0000, v56\n\t"
"v_fmac_f32 v19, v21, v22\n\t"
"v_fmac_f32 v20, v23, v24\n\t"
"v_lshlrev_b32 v21, 16, v41\n\t"
"v_lshlrev_b32 v22, 16, v57\n\t"
"v_and_b32 v23, 0xffff0000, v41\n\t"
"v_and_b32 v24, 0xffff0000, v57\n\t"
"v_fmac_f32 v19, v21, v22\n\t"
"v_fmac_f32 v20, v23, v24\n\t"
"v_lshlrev_b32 v21, 16, v42\n\t"
"v_lshlrev_b32 v22, 16, v58\n\t"
"v_and_b32 v23, 0xffff0000, v42\n\t"
"v_and_b32 v24, 0xffff0000, v58\n\t"
"v_fmac_f32 v19, v21, v22\n\t"
"v_fmac_f32 v20, v23, v24\n\t"
"v_lshlrev_b32 v21, 16, v43\n\t"
"v_lshlrev_b32 v22, 16, v59\n\t"
"v_and_b32 v23, 0xffff0000, v43\n\t"
"v_and_b32 v24, 0xffff0000, v59\n\t"
"v_fmac_f32 v19, v21, v22\n\t"
"v_fmac_f32 v20, v23, v24\n\t"
"v_lshlrev_b32 v21, 16, v44\n\t"
"v_lshlrev_b32 v22, 16, v60\n\t"
"v_and_b32 v23, 0xffff0000, v44\n\t"
"v_and_b32 v24, 0xffff0000, v60\n\t"
"v_fmac_f32 v19, v21, v22\n\t"
"v_fmac_f32 v20, v23, v24\n\t"
"v_lshlrev_b32 v21, 16, v45\n\t"
"v_lshlrev_b32 v22, 16, v61\n\t"
"v_and_b32 v23, 0xffff0000, v45\n\t"
"v_and_b32 v24, 0xffff0000, v61\n\t"
"v_fmac_f32 v19, v21, v22\n\t"
"v_fmac_f32 v20, v23, v24\n\t"
"v_lshlrev_b32 v21, 16, v46\n\t"
"v_lshlrev_b32 v22, 16, v62\n\t"
"v_and_b32 v23, 0xffff0000, v46\n\t"
"v_and_b32 v24, 0xffff0000, v62\n\t"
"v_fmac_f32 v19, v21, v22\n\t"
"v_fmac_f32 v20, v23, v24\n\t"
"v_lshlrev_b32 v21, 16, v47\n\t"
"v_lshlrev_b32 v22, 16, v63\n\t"
"v_and_b32 v23, 0xffff0000, v47\n\t"
"v_and_b32 v24, 0xffff0000, v63\n\t"
"v_fmac_f32 v19, v21, v22\n\t"
"v_fmac_f32 v20, v23, v24\n\t"
"v_add_f32 v19, v19, v20\n\t"
"s_nop 1\n\t"
"v_add_f32_dpp v19, v19, v19 quad_perm:[1,0,3,2] row_mask:0xf bank_mask:0xf\n\t"
"s_nop 1\n\t"
"v_add_f32_dpp v19, v19, v19 quad_perm:[2,3,0,1] row_mask:0xf bank_mask:0xf\n\t"
"s_nop 1\n\t"
"v_add_f32_dpp v19, v19, v19 row_half_mirror row_mask:0xf bank_mask:0xf\n\t"
"s_nop 1\n\t"
"v_add_f32_dpp v19, v19, v19 row_mirror row_mask:0xf bank_mask:0xf\n\t"
"s_nop 1\n\t"
"v_mov_b32 v20, v19\n\t"
"s_nop 1\n\t"
"v_permlane16_swap_b32 v19, v20\n\t"
"s_nop 1\n\t"
"v_add_f32 v19, v19, v20\n\t"
"v_mul_f32 v19, 0xbfb8aa3b, v19\n\t"
"v_min_f32 v19, 0x42640000, v19\n\t"
"v_exp_f32 v19, v19\n\t"
"s_nop 0\n\t"
"v_add_f32 v19, 1.0, v19\n\t"
"v_rcp_f32 v19, v19\n\t"
"v_lshlrev_b32 v17, 2, v17\n\t"
"v_add_u32 v17, 33554432, v17\n\t"
"s_mov_b32 exec_lo, 1\n\t"
"s_mov_b32 exec_hi, 1\n\t"
"buffer_store_dword v19, v17, s[24:27], 0 offen sc1\n\t"
"s_mov_b64 exec, -1\n\t"
"s_waitcnt lgkmcnt(0)\n\t"
"s_mul_i32 s53, s54, 0x9E3779B1\n\t"
"s_lshr_b32 s61, s53, 15\n\t"
"s_xor_b32 s53, s53, s61\n\t"
"s_mul_i32 s53, s53, 0x85EBCA6B\n\t"
"s_xor_b32 s53, s53, 0x5bd1e995\n\t"
"s_barrier\n\t"
"v_lshrrev_b32 v16, 3, v252\n\t"
"v_and_b32 v17, 7, v252\n\t"
"s_lshl_b32 s59, s45, 6\n\t"
"s_lshl_b32 s60, s51, 7\n\t"
"s_xor_b32 s59, s59, s60\n\t"
"s_lshl_b32 s60, s49, 8\n\t"
"s_add_u32 s59, s59, s60\n\t"
"s_lshl_b32 s59, s59, 12\n\t"
"s_lshl_b32 s60, s51, 11\n\t"
"s_add_u32 s59, s59, s60\n\t"
"v_lshrrev_b32 v19, 1, v16\n\t"
"v_add_u32 v19, 0, v19\n\t"
"v_xor_b32 v19, v19, v17\n\t"
"v_add_u32 v18, 0, v16\n\t"
"v_lshlrev_b32 v18, 12, v18\n\t"
"v_lshl_add_u32 v0, v19, 4, v18\n\t"
"s_sub_u32 s60, s59, 0\n\t"
"v_add_u32 v0, s60, v0\n\t"
"v_lshrrev_b32 v19, 1, v16\n\t"
"v_add_u32 v19, 4, v19\n\t"
"v_xor_b32 v19, v19, v17\n\t"
"v_add_u32 v18, 8, v16\n\t"
"v_lshlrev_b32 v18, 12, v18\n\t"
"v_lshl_add_u32 v1, v19, 4, v18\n\t"
"s_sub_u32 s60, s59, 1024\n\t"
"v_add_u32 v1, s60, v1\n\t"
"v_lshrrev_b32 v19, 1, v16\n\t"
"v_add_u32 v19, 0, v19\n\t"
"v_xor_b32 v19, v19, v17\n\t"
"v_add_u32 v18, 16, v16\n\t"
"v_lshlrev_b32 v18, 12, v18\n\t"
"v_lshl_add_u32 v2, v19, 4, v18\n\t"
"s_sub_u32 s60, s59, 2048\n\t"
"v_add_u32 v2, s60, v2\n\t"
"v_lshrrev_b32 v19, 1, v16\n\t"
"v_add_u32 v19, 4, v19\n\t"
"v_xor_b32 v19, v19, v17\n\t"
"v_add_u32 v18, 24, v16\n\t"
"v_lshlrev_b32 v18, 12, v18\n\t"
"v_lshl_add_u32 v3, v19, 4, v18\n\t"
"s_sub_u32 s60, s59, 3072\n\t"
"v_add_u32 v3, s60, v3\n\t"
"v_lshrrev_b32 v19, 1, v16\n\t"
"v_add_u32 v19, 0, v19\n\t"
"v_xor_b32 v19, v19, v17\n\t"
"v_add_u32 v18, 32, v16\n\t"
"v_lshlrev_b32 v18, 12, v18\n\t"
"v_lshl_add_u32 v4, v19, 4, v18\n\t"
"s_sub_u32 s60, s59, 0\n\t"
"v_add_u32 v4, s60, v4\n\t"
"v_lshrrev_b32 v19, 1, v16\n\t"
"v_add_u32 v19, 4, v19\n\t"
"v_xor_b32 v19, v19, v17\n\t"
"v_add_u32 v18, 40, v16\n\t"
"v_lshlrev_b32 v18, 12, v18\n\t"
"v_lshl_add_u32 v5, v19, 4, v18\n\t"
"s_sub_u32 s60, s59, 1024\n\t"
"v_add_u32 v5, s60, v5\n\t"
"v_lshrrev_b32 v19, 1, v16\n\t"
"v_add_u32 v19, 0, v19\n\t"
"v_xor_b32 v19, v19, v17\n\t"
"v_add_u32 v18, 48, v16\n\t"
"v_lshlrev_b32 v18, 12, v18\n\t"
"v_lshl_add_u32 v6, v19, 4, v18\n\t"
"s_sub_u32 s60, s59, 2048\n\t"
"v_add_u32 v6, s60, v6\n\t"
"v_lshrrev_b32 v19, 1, v16\n\t"
"v_add_u32 v19, 4, v19\n\t"
"v_xor_b32 v19, v19, v17\n\t"
"v_add_u32 v18, 56, v16\n\t"
"v_lshlrev_b32 v18, 12, v18\n\t"
"v_lshl_add_u32 v7, v19, 4, v18\n\t"
"s_sub_u32 s60, s59, 3072\n\t"
"v_add_u32 v7, s60, v7\n\t"
"s_lshl_b32 s59, s52, 1\n\t"
"s_xor_b32 s59, s59, s45\n\t"
"s_lshl_b32 s59, s59, 12\n\t"
"v_lshlrev_b32 v9, 4, v252\n\t"
"v_add_u32 v8, s59, v9\n\t"
"s_lshl_b32 s59, s45, 12\n\t"
"v_add_u32 v9, s59, v9\n\t"
"s_mov_b32 s65, 0\n\t"
"s_lshl_b32 s77, s48, 3\n\t"
"s_add_u32 s77, s77, s50\n\t"
"s_lshl_b32 s77, s77, 18\n\t"
"s_add_u32 s77, s77, 8388608\n\t"
"s_lshl_b32 s62, s45, 13\n\t"
"s_lshl_b32 s63, s45, 12\n\t"
"s_lshl_b32 s64, s45, 11\n\t"
"v_lshlrev_b32 v12, 4, v252\n\t"
"s_lshl_b32 s62, s45, 13\n\t"
"v_add_u32 v12, s62, v12\n\t"
"v_lshlrev_b32 v22, 4, v252\n\t"
"v_add_u32 v23, 98304, v12\n\t"
"s_barrier\n\t"
"ds_read_b128 v[32:35], v23 offset:0\n\t"
"ds_read_b128 v[36:39], v23 offset:1024\n\t"
"ds_read_b128 v[40:43], v23 offset:2048\n\t"
"ds_read_b128 v[44:47], v23 offset:3072\n\t"
"ds_read_b128 v[48:51], v23 offset:4096\n\t"
"ds_read_b128 v[52:55], v23 offset:5120\n\t"
"ds_read_b128 v[56:59], v23 offset:6144\n\t"
"ds_read_b128 v[60:63], v23 offset:7168\n\t"
"s_lshr_b32 s59, s46, 2\n\t"
"s_lshl_b32 s59, s59, 3\n\t"
"s_lshr_b32 s60, s47, 2\n\t"
"s_add_u32 s59, s59, s60\n\t"
"s_lshl_b32 s59, s59, 4\n\t"
"s_and_b32 s60, s46, 3\n\t"
"s_lshl_b32 s60, s60, 2\n\t"
"s_add_u32 s59, s59, s60\n\t"
"s_add_u32 s59, s59, s45\n\t"
"s_lshl_b32 s59, s59, 14\n\t"
"s_and_b32 s60, s47, 3\n\t"
"s_lshl_b32 s60, s60, 12\n\t"
"s_add_u32 s59, s59, s60\n\t"
"s_add_u32 s70, s59, 8388608\n\t"
"s_add_u32 s71, s70, 4194304\n\t"
"s_lshl_b32 s60, s47, 2\n\t"
"s_add_u32 s60, s60, s45\n\t"
"s_lshl_b32 s60, s60, 10\n\t"
"s_add_u32 s60, s60, 33685504\n\t"
"v_lshlrev_b32 v13, 7, v252\n\t"
"v_add_u32 v13, s60, v13\n\t"
"s_waitcnt lgkmcnt(0)\n\t"
"s_add_u32 s79, s46, 1\n\t"
"s_and_b32 s79, s79, 7\n\t"
"s_lshl_b32 s59, s79, 7\n\t"
"s_mov_b64 exec, 1\n\t"
"s_mov_b32 s78, 0x400000\n\t"
"L_pkv1_%=:\n\t"
"buffer_load_dword v20, v13, s[24:27], s59 offen sc1\n\t"
"s_waitcnt vmcnt(0)\n\t"
"v_cmp_ne_u32 vcc, s53, v20\n\t"
"s_nop 4\n\t"
"s_cbranch_vccz L_pkv1_ok_%=\n\t"
"s_sleep 1\n\t"
"s_sub_u32 s78, s78, 1\n\t"
"s_cmp_lg_u32 s78, 0\n\t"
"s_cbranch_scc1 L_pkv1_%=\n\t"
"L_pkv1_ok_%=:\n\t"
"s_mov_b64 exec, -1\n\t"
"s_add_u32 s59, s46, 1\n\t"
"s_and_b32 s59, s59, 7\n\t"
"s_lshl_b32 s60, s47, 3\n\t"
"s_add_u32 s59, s59, s60\n\t"
"s_lshl_b32 s59, s59, 15\n\t"
"s_add_u32 s60, s59, 4096\n\t"
"s_add_u32 m0, s62, 32768\n\t"
"s_nop 0\n\t"
"buffer_load_dwordx4 v12, s[24:27], s59 offen sc1 lds\n\t"
"buffer_load_dwordx4 v12, s[24:27], s59 offen offset:1024 sc1 lds\n\t"
"buffer_load_dwordx4 v12, s[24:27], s59 offen offset:2048 sc1 lds\n\t"
"buffer_load_dwordx4 v12, s[24:27], s59 offen offset:3072 sc1 lds\n\t"
"s_add_u32 m0, m0, 4096\n\t"
"s_nop 0\n\t"
"buffer_load_dwordx4 v12, s[24:27], s60 offen sc1 lds\n\t"
"buffer_load_dwordx4 v12, s[24:27], s60 offen offset:1024 sc1 lds\n\t"
"buffer_load_dwordx4 v12, s[24:27], s60 offen offset:2048 sc1 lds\n\t"
"buffer_load_dwordx4 v12, s[24:27], s60 offen offset:3072 sc1 lds\n\t"
"s_waitcnt vmcnt(0)\n\t"
"s_barrier\n\t"
"s_add_u32 s79, s46, 2\n\t"
"s_and_b32 s79, s79, 7\n\t"
"s_lshl_b32 s59, s79, 7\n\t"
"s_mov_b64 exec, 1\n\t"
"s_mov_b32 s78, 0x400000\n\t"
"L_pkv2_%=:\n\t"
"buffer_load_dword v20, v13, s[24:27], s59 offen sc1\n\t"
"s_waitcnt vmcnt(0)\n\t"
"v_cmp_ne_u32 vcc, s53, v20\n\t"
"s_nop 4\n\t"
"s_cbranch_vccz L_pkv2_ok_%=\n\t"
"s_sleep 1\n\t"
"s_sub_u32 s78, s78, 1\n\t"
"s_cmp_lg_u32 s78, 0\n\t"
"s_cbranch_scc1 L_pkv2_%=\n\t"
"L_pkv2_ok_%=:\n\t"
"s_mov_b64 exec, -1\n\t"
"s_add_u32 s59, s46, 2\n\t"
"s_and_b32 s59, s59, 7\n\t"
"s_lshl_b32 s60, s47, 3\n\t"
"s_add_u32 s59, s59, s60\n\t"
"s_lshl_b32 s59, s59, 15\n\t"
"s_add_u32 s60, s59, 4096\n\t"
"s_add_u32 m0, s62, 65536\n\t"
"s_nop 0\n\t"
"buffer_load_dwordx4 v12, s[24:27], s59 offen sc1 lds\n\t"
"buffer_load_dwordx4 v12, s[24:27], s59 offen offset:1024 sc1 lds\n\t"
"buffer_load_dwordx4 v12, s[24:27], s59 offen offset:2048 sc1 lds\n\t"
"buffer_load_dwordx4 v12, s[24:27], s59 offen offset:3072 sc1 lds\n\t"
"s_add_u32 m0, m0, 4096\n\t"
"s_nop 0\n\t"
"buffer_load_dwordx4 v12, s[24:27], s60 offen sc1 lds\n\t"
"buffer_load_dwordx4 v12, s[24:27], s60 offen offset:1024 sc1 lds\n\t"
"buffer_load_dwordx4 v12, s[24:27], s60 offen offset:2048 sc1 lds\n\t"
"buffer_load_dwordx4 v12, s[24:27], s60 offen offset:3072 sc1 lds\n\t"
"s_mov_b32 exec_lo, 255\n\t"
"s_mov_b32 exec_hi, 0\n\t"
"s_mov_b32 s78, 0x400000\n\t"
"L_pkva_%=:\n\t"
"buffer_load_dword v20, v13, s[24:27], 0 offen sc1\n\t"
"s_waitcnt vmcnt(0)\n\t"
"v_cmp_ne_u32 vcc, s53, v20\n\t"
"s_nop 4\n\t"
"s_cbranch_vccz L_pkva_ok_%=\n\t"
"s_sleep 2\n\t"
"s_sub_u32 s78, s78, 1\n\t"
"s_cmp_lg_u32 s78, 0\n\t"
"s_cbranch_scc1 L_pkva_%=\n\t"
"L_pkva_ok_%=:\n\t"
"s_mov_b64 exec, -1\n\t"
"s_add_u32 s59, s46, 3\n\t"
"s_and_b32 s59, s59, 7\n\t"
"s_lshl_b32 s60, s47, 3\n\t"
"s_add_u32 s59, s59, s60\n\t"
"s_lshl_b32 s59, s59, 15\n\t"
"s_add_u32 s60, s59, 4096\n\t"
"s_add_u32 m0, s62, 98304\n\t"
"s_nop 0\n\t"
"buffer_load_dwordx4 v12, s[24:27], s59 offen sc1 lds\n\t"
"buffer_load_dwordx4 v12, s[24:27], s59 offen offset:1024 sc1 lds\n\t"
"buffer_load_dwordx4 v12, s[24:27], s59 offen offset:2048 sc1 lds\n\t"
"buffer_load_dwordx4 v12, s[24:27], s59 offen offset:3072 sc1 lds\n\t"
"s_add_u32 m0, m0, 4096\n\t"
"s_nop 0\n\t"
"buffer_load_dwordx4 v12, s[24:27], s60 offen sc1 lds\n\t"
"buffer_load_dwordx4 v12, s[24:27], s60 offen offset:1024 sc1 lds\n\t"
"buffer_load_dwordx4 v12, s[24:27], s60 offen offset:2048 sc1 lds\n\t"
"buffer_load_dwordx4 v12, s[24:27], s60 offen offset:3072 sc1 lds\n\t"
"s_lshr_b32 s59, s55, 6\n\t"
"s_lshl_b32 s59, s59, 3\n\t"
"s_and_b32 s60, s55, 7\n\t"
"s_add_u32 s59, s59, s60\n\t"
"s_lshl_b32 s59, s59, 16\n\t"
"s_bfe_u32 s60, s55, 0x30003\n\t"
"s_lshl_b32 s60, s60, 8\n\t"
"s_add_u32 s59, s59, s60\n\t"
"s_lshl_b32 s60, s45, 6\n\t"
"s_add_u32 s59, s59, s60\n\t"
"s_lshl_b32 s60, s45, 12\n\t"
"s_add_u32 m0, s60, 147456\n\t"
"s_add_u32 s60, s59, 31744\n\t"
"buffer_load_dwordx4 v250, s[32:35], s59 offen lds\n\t"
"buffer_load_dwordx4 v250, s[32:35], s60 offen offset:1024 lds\n\t"
"s_mov_b32 exec_lo, 0xeeeeeeee\n\t"
"s_mov_b32 exec_hi, 0\n\t"
"buffer_load_dword v23, v15, s[16:19], 0 offen offset:1536\n\t"
"s_mov_b64 exec, -1\n\t"
"s_barrier\n\t"
"s_add_u32 s59, s46, 4\n\t"
"s_and_b32 s59, s59, 7\n\t"
"s_lshl_b32 s60, s47, 3\n\t"
"s_add_u32 s59, s59, s60\n\t"
"s_lshl_b32 s59, s59, 15\n\t"
"s_add_u32 s60, s59, 4096\n\t"
"s_add_u32 m0, s62, 0\n\t"
"s_nop 0\n\t"
"buffer_load_dwordx4 v12, s[24:27], s59 offen sc1 lds\n\t"
"buffer_load_dwordx4 v12, s[24:27], s59 offen offset:1024 sc1 lds\n\t"
"buffer_load_dwordx4 v12, s[24:27], s59 offen offset:2048 sc1 lds\n\t"
"buffer_load_dwordx4 v12, s[24:27], s59 offen offset:3072 sc1 lds\n\t"
"s_add_u32 m0, m0, 4096\n\t"
"s_nop 0\n\t"
"buffer_load_dwordx4 v12, s[24:27], s60 offen sc1 lds\n\t"
"buffer_load_dwordx4 v12, s[24:27], s60 offen offset:1024 sc1 lds\n\t"
"buffer_load_dwordx4 v12, s[24:27], s60 offen offset:2048 sc1 lds\n\t"
"buffer_load_dwordx4 v12, s[24:27], s60 offen offset:3072 sc1 lds\n\t"
"buffer_store_dwordx4 v[32:35], v22, s[24:27], s70 offen sc1\n\t"
"s_sleep 2\n\t"
"buffer_store_dwordx4 v[36:39], v22, s[24:27], s70 offen offset:1024 sc1\n\t"
"s_sleep 2\n\t"
"buffer_store_dwordx4 v[40:43], v22, s[24:27], s70 offen offset:2048 sc1\n\t"
"s_sleep 2\n\t"
"buffer_store_dwordx4 v[44:47], v22, s[24:27], s70 offen offset:3072 sc1\n\t"
"s_sleep 2\n\t"
"buffer_store_dwordx4 v[48:51], v22, s[24:27], s71 offen sc1\n\t"
"s_sleep 2\n\t"
"buffer_store_dwordx4 v[52:55], v22, s[24:27], s71 offen offset:1024 sc1\n\t"
"s_sleep 2\n\t"
"s_lshr_b32 s59, s55, 1\n\t"
"s_lshl_b32 s59, s59, 14\n\t"
"s_and_b32 s60, s55, 1\n\t"
"s_lshl_b32 s60, s60, 10\n\t"
"s_add_u32 s59, s59, s60\n\t"
"s_lshl_b32 s60, s45, 12\n\t"
"s_add_u32 s59, s59, s60\n\t"
"s_add_u32 s59, s59, 41943040\n\t"
"s_waitcnt vmcnt(15)\n\t"
"ds_read_b64_tr_b16 v[24:25], v251 offset:0\n\t"
"ds_read_b64_tr_b16 v[26:27], v251 offset:512\n\t"
"ds_read_b64_tr_b16 v[28:29], v10 offset:0\n\t"
"ds_read_b64_tr_b16 v[30:31], v10 offset:512\n\t"
"s_waitcnt lgkmcnt(0)\n\t"
"s_mov_b32 s68, s59\n\t"
"s_add_u32 m0, s63, 131072\n\t"
"s_mov_b32 s59, 1664\n\t"
"buffer_load_dwordx4 v244, s[16:19], s59 offen lds\n\t"
"buffer_load_dwordx4 v245, s[16:19], s59 offen offset:1024 lds\n\t"
"buffer_load_dwordx4 v246, s[16:19], s59 offen offset:2048 lds\n\t"
"buffer_load_dwordx4 v247, s[16:19], s59 offen offset:3072 lds\n\t"
"s_add_u32 m0, s63, 147456\n\t"
"s_mov_b32 s59, 1792\n\t"
"buffer_load_dwordx4 v244, s[16:19], s59 offen lds\n\t"
"buffer_load_dwordx4 v245, s[16:19], s59 offen offset:1024 lds\n\t"
"buffer_load_dwordx4 v246, s[16:19], s59 offen offset:2048 lds\n\t"
"buffer_load_dwordx4 v247, s[16:19], s59 offen offset:3072 lds\n\t"
"s_mov_b32 exec_lo, 0xeeeeeeee\n\t"
"s_mov_b32 exec_hi, 0\n\t"
"buffer_load_dword v23, v15, s[20:23], 0 offen offset:1536\n\t"
"s_mov_b64 exec, -1\n\t"
"s_barrier\n\t"
"s_add_u32 s59, s46, 5\n\t"
"s_and_b32 s59, s59, 7\n\t"
"s_lshl_b32 s60, s47, 3\n\t"
"s_add_u32 s59, s59, s60\n\t"
"s_lshl_b32 s59, s59, 15\n\t"
"s_add_u32 s60, s59, 4096\n\t"
"s_add_u32 m0, s62, 32768\n\t"
"s_nop 0\n\t"
"buffer_load_dwordx4 v12, s[24:27], s59 offen sc1 lds\n\t"
"buffer_load_dwordx4 v12, s[24:27], s59 offen offset:1024 sc1 lds\n\t"
"buffer_load_dwordx4 v12, s[24:27], s59 offen offset:2048 sc1 lds\n\t"
"buffer_load_dwordx4 v12, s[24:27], s59 offen offset:3072 sc1 lds\n\t"
"s_add_u32 m0, m0, 4096\n\t"
"s_nop 0\n\t"
"buffer_load_dwordx4 v12, s[24:27], s60 offen sc1 lds\n\t"
"buffer_load_dwordx4 v12, s[24:27], s60 offen offset:1024 sc1 lds\n\t"
"buffer_load_dwordx4 v12, s[24:27], s60 offen offset:2048 sc1 lds\n\t"
"buffer_load_dwordx4 v12, s[24:27], s60 offen offset:3072 sc1 lds\n\t"
"s_waitcnt vmcnt(9)\n\t"
"ds_read_b128 v[64:67], v248 offset:0\n\t"
"ds_read_b128 v[68:71], v248 offset:2048\n\t"
"ds_read_b128 v[72:75], v249 offset:0\n\t"
"ds_read_b128 v[76:79], v249 offset:2048\n\t"
"ds_read_b128 v[80:83], v248 offset:16384\n\t"
"ds_read_b128 v[84:87], v248 offset:18432\n\t"
"ds_read_b128 v[88:91], v249 offset:16384\n\t"
"ds_read_b128 v[92:95], v249 offset:18432\n\t"
"s_waitcnt lgkmcnt(0)\n\t"
"s_add_u32 m0, s63, 131072\n\t"
"s_mov_b32 s59, 1920\n\t"
"buffer_load_dwordx4 v244, s[16:19], s59 offen lds\n\t"
"buffer_load_dwordx4 v245, s[16:19], s59 offen offset:1024 lds\n\t"
"buffer_load_dwordx4 v246, s[16:19], s59 offen offset:2048 lds\n\t"
"buffer_load_dwordx4 v247, s[16:19], s59 offen offset:3072 lds\n\t"
"s_add_u32 m0, s63, 147456\n\t"
"s_mov_b32 s59, 3712\n\t"
"buffer_load_dwordx4 v244, s[16:19], s59 offen lds\n\t"
"buffer_load_dwordx4 v245, s[16:19], s59 offen offset:1024 lds\n\t"
"buffer_load_dwordx4 v246, s[16:19], s59 offen offset:2048 lds\n\t"
"buffer_load_dwordx4 v247, s[16:19], s59 offen offset:3072 lds\n\t"
"buffer_store_dwordx4 v[56:59], v22, s[24:27], s71 offen offset:2048 sc1\n\t"
"s_sleep 1\n\t"
"buffer_store_dwordx4 v[60:63], v22, s[24:27], s71 offen offset:3072 sc1\n\t"
"s_sleep 1\n\t"
"buffer_store_dwordx4 v[24:27], v22, s[24:27], s68 offen sc1\n\t"
"s_sleep 1\n\t"
"buffer_store_dwordx4 v[28:31], v22, s[24:27], s68 offen offset:2048 sc1\n\t"
"s_sleep 1\n\t"
"s_lshl_b32 s59, s49, 3\n\t"
"s_add_u32 s59, s59, s50\n\t"
"s_lshl_b32 s59, s59, 2\n\t"
"s_add_u32 s59, s59, s48\n\t"
"s_mul_i32 s59, s59, 49152\n\t"
"s_add_u32 s59, s59, s63\n\t"
"s_add_u32 s69, s59, 20971520\n\t"
"v_mov_b32 v16, -1\n\t"
"v_mov_b32 v17, -1\n\t"
"v_mov_b32 v18, -1\n\t"
"v_mov_b32 v19, -1\n\t"
"buffer_store_dwordx4 v[16:19], v22, s[24:27], s69 offen offset:0 sc1\n\t"
"s_sleep 1\n\t"
"buffer_store_dwordx4 v[16:19], v22, s[24:27], s69 offen offset:1024 sc1\n\t"
"s_sleep 1\n\t"
"s_mov_b32 exec_lo, -1\n\t"
"s_mov_b32 exec_hi, 0\n\t"
"buffer_load_dword v23, v14, s[28:31], 0 offen offset:0\n\t"
"s_mov_b64 exec, -1\n\t"
"s_barrier\n\t"
"s_add_u32 s59, s46, 6\n\t"
"s_and_b32 s59, s59, 7\n\t"
"s_lshl_b32 s60, s47, 3\n\t"
"s_add_u32 s59, s59, s60\n\t"
"s_lshl_b32 s59, s59, 15\n\t"
"s_add_u32 s60, s59, 4096\n\t"
"s_add_u32 m0, s62, 65536\n\t"
"s_nop 0\n\t"
"buffer_load_dwordx4 v12, s[24:27], s59 offen sc1 lds\n\t"
"buffer_load_dwordx4 v12, s[24:27], s59 offen offset:1024 sc1 lds\n\t"
"buffer_load_dwordx4 v12, s[24:27], s59 offen offset:2048 sc1 lds\n\t"
"buffer_load_dwordx4 v12, s[24:27], s59 offen offset:3072 sc1 lds\n\t"
"s_add_u32 m0, m0, 4096\n\t"
"s_nop 0\n\t"
"buffer_load_dwordx4 v12, s[24:27], s60 offen sc1 lds\n\t"
"buffer_load_dwordx4 v12, s[24:27], s60 offen offset:1024 sc1 lds\n\t"
"buffer_load_dwordx4 v12, s[24:27], s60 offen offset:2048 sc1 lds\n\t"
"buffer_load_dwordx4 v12, s[24:27], s60 offen offset:3072 sc1 lds\n\t"
"s_waitcnt vmcnt(15)\n\t"
"ds_read_b128 v[96:99], v248 offset:0\n\t"
"ds_read_b128 v[100:103], v248 offset:2048\n\t"
"ds_read_b128 v[104:107], v249 offset:0\n\t"
"ds_read_b128 v[108:111], v249 offset:2048\n\t"
"ds_read_b128 v[112:115], v248 offset:16384\n\t"
"ds_read_b128 v[116:119], v248 offset:18432\n\t"
"ds_read_b128 v[120:123], v249 offset:16384\n\t"
"ds_read_b128 v[124:127], v249 offset:18432\n\t"
"s_waitcnt lgkmcnt(0)\n\t"
"s_add_u32 m0, s63, 131072\n\t"
"s_mov_b32 s59, 3840\n\t"
"buffer_load_dwordx4 v244, s[16:19], s59 offen lds\n\t"
"buffer_load_dwordx4 v245, s[16:19], s59 offen offset:1024 lds\n\t"
"buffer_load_dwordx4 v246, s[16:19], s59 offen offset:2048 lds\n\t"
"buffer_load_dwordx4 v247, s[16:19], s59 offen offset:3072 lds\n\t"
"s_add_u32 m0, s63, 147456\n\t"
"s_mov_b32 s59, 3968\n\t"
"buffer_load_dwordx4 v244, s[16:19], s59 offen lds\n\t"
"buffer_load_dwordx4 v245, s[16:19], s59 offen offset:1024 lds\n\t"
"buffer_load_dwordx4 v246, s[16:19], s59 offen offset:2048 lds\n\t"
"buffer_load_dwordx4 v247, s[16:19], s59 offen offset:3072 lds\n\t"
"buffer_store_dwordx4 v[16:19], v22, s[24:27], s69 offen offset:2048 sc1\n\t"
"s_sleep 1\n\t"
"buffer_store_dwordx4 v[16:19], v22, s[24:27], s69 offen offset:3072 sc1\n\t"
"s_sleep 1\n\t"
"s_add_u32 s69, s69, 16384\n\t"
"buffer_store_dwordx4 v[16:19], v22, s[24:27], s69 offen offset:0 sc1\n\t"
"s_sleep 1\n\t"
"buffer_store_dwordx4 v[16:19], v22, s[24:27], s69 offen offset:1024 sc1\n\t"
"s_sleep 1\n\t"
"buffer_store_dwordx4 v[16:19], v22, s[24:27], s69 offen offset:2048 sc1\n\t"
"s_sleep 1\n\t"
"buffer_store_dwordx4 v[16:19], v22, s[24:27], s69 offen offset:3072 sc1\n\t"
"s_sleep 1\n\t"
"s_add_u32 s69, s69, 16384\n\t"
"s_mov_b32 exec_lo, -1\n\t"
"s_mov_b32 exec_hi, 0\n\t"
"buffer_load_dword v23, v14, s[28:31], 0 offen offset:512\n\t"
"s_mov_b64 exec, -1\n\t"
"s_waitcnt vmcnt(26)\n\t"
"s_barrier\n\t"
"s_lshl_b32 s59, s47, 3\n\t"
"s_add_u32 s59, s59, s46\n\t"
"s_lshl_b32 s59, s59, 2\n\t"
"s_add_u32 s59, s59, 33570816\n\t"
"s_cmp_lg_u32 s45, 0\n\t"
"s_cbranch_scc1 L_nopub1_%=\n\t"
"v_mov_b32 v20, s53\n\t"
"v_mov_b32 v21, 0\n\t"
"s_mov_b64 exec, 1\n\t"
"buffer_store_dword v20, v21, s[24:27], s59 offen sc1\n\t"
"s_mov_b64 exec, -1\n\t"
"L_nopub1_%=:\n\t"
"s_add_u32 s59, s46, 7\n\t"
"s_and_b32 s59, s59, 7\n\t"
"s_lshl_b32 s60, s47, 3\n\t"
"s_add_u32 s59, s59, s60\n\t"
"s_lshl_b32 s59, s59, 15\n\t"
"s_add_u32 s60, s59, 4096\n\t"
"s_add_u32 m0, s62, 98304\n\t"
"s_nop 0\n\t"
"buffer_load_dwordx4 v12, s[24:27], s59 offen sc1 lds\n\t"
"buffer_load_dwordx4 v12, s[24:27], s59 offen offset:1024 sc1 lds\n\t"
"buffer_load_dwordx4 v12, s[24:27], s59 offen offset:2048 sc1 lds\n\t"
"buffer_load_dwordx4 v12, s[24:27], s59 offen offset:3072 sc1 lds\n\t"
"s_add_u32 m0, m0, 4096\n\t"
"s_nop 0\n\t"
"buffer_load_dwordx4 v12, s[24:27], s60 offen sc1 lds\n\t"
"buffer_load_dwordx4 v12, s[24:27], s60 offen offset:1024 sc1 lds\n\t"
"buffer_load_dwordx4 v12, s[24:27], s60 offen offset:2048 sc1 lds\n\t"
"buffer_load_dwordx4 v12, s[24:27], s60 offen offset:3072 sc1 lds\n\t"
"s_waitcnt vmcnt(15)\n\t"
"ds_read_b128 v[128:131], v248 offset:0\n\t"
"ds_read_b128 v[132:135], v248 offset:2048\n\t"
"ds_read_b128 v[136:139], v249 offset:0\n\t"
"ds_read_b128 v[140:143], v249 offset:2048\n\t"
"ds_read_b128 v[144:147], v248 offset:16384\n\t"
"ds_read_b128 v[148:151], v248 offset:18432\n\t"
"ds_read_b128 v[152:155], v249 offset:16384\n\t"
"ds_read_b128 v[156:159], v249 offset:18432\n\t"
"s_waitcnt lgkmcnt(0)\n\t"
"s_add_u32 m0, s63, 131072\n\t"
"s_mov_b32 s59, 1664\n\t"
"buffer_load_dwordx4 v244, s[20:23], s59 offen lds\n\t"
"buffer_load_dwordx4 v245, s[20:23], s59 offen offset:1024 lds\n\t"
"buffer_load_dwordx4 v246, s[20:23], s59 offen offset:2048 lds\n\t"
"buffer_load_dwordx4 v247, s[20:23], s59 offen offset:3072 lds\n\t"
"s_add_u32 m0, s63, 147456\n\t"
"s_mov_b32 s59, 1792\n\t"
"buffer_load_dwordx4 v244, s[20:23], s59 offen lds\n\t"
"buffer_load_dwordx4 v245, s[20:23], s59 offen offset:1024 lds\n\t"
"buffer_load_dwordx4 v246, s[20:23], s59 offen offset:2048 lds\n\t"
"buffer_load_dwordx4 v247, s[20:23], s59 offen offset:3072 lds\n\t"
"buffer_store_dwordx4 v[16:19], v22, s[24:27], s69 offen offset:0 sc1\n\t"
"s_sleep 1\n\t"
"buffer_store_dwordx4 v[16:19], v22, s[24:27], s69 offen offset:1024 sc1\n\t"
"s_sleep 1\n\t"
"buffer_store_dwordx4 v[16:19], v22, s[24:27], s69 offen offset:2048 sc1\n\t"
"s_sleep 1\n\t"
"buffer_store_dwordx4 v[16:19], v22, s[24:27], s69 offen offset:3072 sc1\n\t"
"s_sleep 1\n\t"
"s_add_u32 s69, s69, 16384\n\t"
"v_add_u32 v13, 33570816, v22\n\t"
"s_barrier\n\t"
"s_mov_b32 exec_lo, -1\n\t"
"s_mov_b32 exec_hi, -1\n\t"
"s_mov_b32 s78, 0x400000\n\t"
"L_pall1_%=:\n\t"
"buffer_load_dwordx4 v[24:27], v13, s[24:27], 0 offen sc1\n\t"
"s_waitcnt vmcnt(0)\n\t"
"v_xor_b32 v24, s53, v24\n\t"
"v_xor_b32 v25, s53, v25\n\t"
"v_xor_b32 v26, s53, v26\n\t"
"v_xor_b32 v27, s53, v27\n\t"
"v_or_b32 v24, v24, v25\n\t"
"v_or3_b32 v24, v24, v26, v27\n\t"
"v_cmp_ne_u32 vcc, 0, v24\n\t"
"s_nop 4\n\t"
"s_cbranch_vccz L_pall1_ok_%=\n\t"
"s_sleep 2\n\t"
"s_sub_u32 s78, s78, 1\n\t"
"s_cmp_lg_u32 s78, 0\n\t"
"s_cbranch_scc1 L_pall1_%=\n\t"
"L_pall1_ok_%=:\n\t"
"s_mov_b64 exec, -1\n\t"
"s_cmp_lg_u32 s45, 0\n\t"
"s_cbranch_scc1 L_cpb_%=\n\t"
"s_getpc_b64 s[96:97]\n\t"
"L_pcb_%=:\n\t"
"s_sub_u32 s96, s96, L_pcb_%=-L_start_%=\n\t"
"s_subb_u32 s97, s97, 0\n\t"
"s_and_b32 s97, s97, 0xffff\n\t"
"s_mov_b32 s98, L_end_%=-L_start_%=\n\t"
"s_mov_b32 s99, 0x20000\n\t"
"s_lshl_b32 s59, s4, 11\n\t"
"v_lshlrev_b32 v21, 7, v253\n\t"
"buffer_load_dword v21, v21, s[96:99], s59 offen\n\t"
"L_cpb_%=:\n\t"
"s_waitcnt vmcnt(0)\n\t"
"ds_read_b128 v[160:163], v248 offset:0\n\t"
"ds_read_b128 v[164:167], v248 offset:2048\n\t"
"ds_read_b128 v[168:171], v249 offset:0\n\t"
"ds_read_b128 v[172:175], v249 offset:2048\n\t"
"ds_read_b128 v[176:179], v248 offset:16384\n\t"
"ds_read_b128 v[180:183], v248 offset:18432\n\t"
"ds_read_b128 v[184:187], v249 offset:16384\n\t"
"ds_read_b128 v[188:191], v249 offset:18432\n\t"
"s_waitcnt lgkmcnt(0)\n\t"
"s_add_u32 m0, s63, 131072\n\t"
"s_mov_b32 s59, 1920\n\t"
"buffer_load_dwordx4 v244, s[20:23], s59 offen lds\n\t"
"buffer_load_dwordx4 v245, s[20:23], s59 offen offset:1024 lds\n\t"
"buffer_load_dwordx4 v246, s[20:23], s59 offen offset:2048 lds\n\t"
"buffer_load_dwordx4 v247, s[20:23], s59 offen offset:3072 lds\n\t"
"s_add_u32 m0, s63, 147456\n\t"
"s_mov_b32 s59, 3712\n\t"
"buffer_load_dwordx4 v244, s[20:23], s59 offen lds\n\t"
"buffer_load_dwordx4 v245, s[20:23], s59 offen offset:1024 lds\n\t"
"buffer_load_dwordx4 v246, s[20:23], s59 offen offset:2048 lds\n\t"
"buffer_load_dwordx4 v247, s[20:23], s59 offen offset:3072 lds\n\t"
"s_add_u32 m0, s62, 0\n\t"
"s_nop 0\n\t"
"buffer_load_dwordx4 v0, s[28:31], s65 offen lds\n\t"
"buffer_load_dwordx4 v1, s[28:31], s65 offen offset:1024 lds\n\t"
"buffer_load_dwordx4 v2, s[28:31], s65 offen offset:2048 lds\n\t"
"buffer_load_dwordx4 v3, s[28:31], s65 offen offset:3072 lds\n\t"
"s_add_u32 m0, m0, 4096\n\t"
"s_nop 0\n\t"
"buffer_load_dwordx4 v4, s[28:31], s65 offen lds\n\t"
"buffer_load_dwordx4 v5, s[28:31], s65 offen offset:1024 lds\n\t"
"buffer_load_dwordx4 v6, s[28:31], s65 offen offset:2048 lds\n\t"
"buffer_load_dwordx4 v7, s[28:31], s65 offen offset:3072 lds\n\t"
"s_add_u32 s65, s65, 128\n\t"
"s_mov_b32 exec_lo, -1\n\t"
"s_mov_b32 exec_hi, 0\n\t"
"buffer_load_dword v23, v14, s[28:31], 0 offen offset:1024\n\t"
"s_mov_b64 exec, -1\n\t"
"s_barrier\n\t"
"s_waitcnt vmcnt(9)\n\t"
"ds_read_b128 v[192:195], v248 offset:0\n\t"
"ds_read_b128 v[196:199], v248 offset:2048\n\t"
"ds_read_b128 v[200:203], v249 offset:0\n\t"
"ds_read_b128 v[204:207], v249 offset:2048\n\t"
"ds_read_b128 v[208:211], v248 offset:16384\n\t"
"ds_read_b128 v[212:215], v248 offset:18432\n\t"
"ds_read_b128 v[216:219], v249 offset:16384\n\t"
"ds_read_b128 v[220:223], v249 offset:18432\n\t"
"s_waitcnt lgkmcnt(0)\n\t"
"s_mov_b32 exec_lo, 0x11111111\n\t"
"s_mov_b32 exec_hi, 0\n\t"
"buffer_load_dword v23, v14, s[28:31], 0 offen offset:1536\n\t"
"s_mov_b64 exec, -1\n\t"
"s_add_u32 m0, s63, 131072\n\t"
"s_nop 0\n\t"
"buffer_load_dwordx4 v8, s[24:27], s77 offen sc1 lds\n\t"
"buffer_load_dwordx4 v8, s[24:27], s77 offen offset:1024 sc1 lds\n\t"
"buffer_load_dwordx4 v8, s[24:27], s77 offen offset:2048 sc1 lds\n\t"
"buffer_load_dwordx4 v8, s[24:27], s77 offen offset:3072 sc1 lds\n\t"
"s_add_u32 s77, s77, 16384\n\t"
"s_add_u32 m0, s62, 32768\n\t"
"s_nop 0\n\t"
"buffer_load_dwordx4 v0, s[28:31], s65 offen lds\n\t"
"buffer_load_dwordx4 v1, s[28:31], s65 offen offset:1024 lds\n\t"
"buffer_load_dwordx4 v2, s[28:31], s65 offen offset:2048 lds\n\t"
"buffer_load_dwordx4 v3, s[28:31], s65 offen offset:3072 lds\n\t"
"s_add_u32 m0, m0, 4096\n\t"
"s_nop 0\n\t"
"buffer_load_dwordx4 v4, s[28:31], s65 offen lds\n\t"
"buffer_load_dwordx4 v5, s[28:31], s65 offen offset:1024 lds\n\t"
"buffer_load_dwordx4 v6, s[28:31], s65 offen offset:2048 lds\n\t"
"buffer_load_dwordx4 v7, s[28:31], s65 offen offset:3072 lds\n\t"
"s_add_u32 s65, s65, 128\n\t"
"s_add_u32 m0, s63, 147456\n\t"
"s_nop 0\n\t"
"buffer_load_dwordx4 v8, s[24:27], s77 offen sc1 lds\n\t"
"buffer_load_dwordx4 v8, s[24:27], s77 offen offset:1024 sc1 lds\n\t"
"buffer_load_dwordx4 v8, s[24:27], s77 offen offset:2048 sc1 lds\n\t"
"buffer_load_dwordx4 v8, s[24:27], s77 offen offset:3072 sc1 lds\n\t"
"s_add_u32 s77, s77, 16384\n\t"
"v_add_u32 v248, 4294950912, v248\n\t"
"v_add_u32 v249, 4294950912, v249\n\t"
"s_barrier\n\t"
"s_waitcnt vmcnt(12)\n\t"
"s_barrier\n\t"
"s_add_u32 m0, s62, 65536\n\t"
"s_nop 0\n\t"
"buffer_load_dwordx4 v0, s[28:31], s65 offen lds\n\t"
"buffer_load_dwordx4 v1, s[28:31], s65 offen offset:1024 lds\n\t"
"buffer_load_dwordx4 v2, s[28:31], s65 offen offset:2048 lds\n\t"
"buffer_load_dwordx4 v3, s[28:31], s65 offen offset:3072 lds\n\t"
"s_add_u32 m0, m0, 4096\n\t"
"s_nop 0\n\t"
"buffer_load_dwordx4 v4, s[28:31], s65 offen lds\n\t"
"buffer_load_dwordx4 v5, s[28:31], s65 offen offset:1024 lds\n\t"
"buffer_load_dwordx4 v6, s[28:31], s65 offen offset:2048 lds\n\t"
"buffer_load_dwordx4 v7, s[28:31], s65 offen offset:3072 lds\n\t"
"s_add_u32 s65, s65, 128\n\t"
"s_add_u32 m0, s63, 98304\n\t"
"s_nop 0\n\t"
"buffer_load_dwordx4 v8, s[24:27], s77 offen sc1 lds\n\t"
"buffer_load_dwordx4 v8, s[24:27], s77 offen offset:1024 sc1 lds\n\t"
"buffer_load_dwordx4 v8, s[24:27], s77 offen offset:2048 sc1 lds\n\t"
"buffer_load_dwordx4 v8, s[24:27], s77 offen offset:3072 sc1 lds\n\t"
"s_add_u32 s77, s77, 16384\n\t"
"s_lshl_b32 s59, s49, 12\n\t"
"s_add_u32 s59, s59, 33619968\n\t"
"v_lshlrev_b32 v13, 7, v252\n\t"
"v_and_b32 v13, 0xfff, v13\n\t"
"v_add_u32 v13, s59, v13\n\t"
"s_lshl_b32 s59, s46, 5\n\t"
"s_and_b32 s60, s47, 7\n\t"
"s_add_u32 s59, s59, s60\n\t"
"s_lshl_b32 s59, s59, 7\n\t"
"s_add_u32 s59, s59, 33619968\n\t"
"v_and_b32 v14, 3, v252\n\t"
"v_lshlrev_b32 v14, 10, v14\n\t"
"v_add_u32 v14, s59, v14\n\t"
"v_cmp_lt_u32 vcc, 31, v252\n\t"
"s_nop 1\n\t"
"v_cndmask_b32 v13, v13, v14, vcc\n\t"
"s_lshr_b32 s59, s45, 1\n\t"
"s_xor_b32 s59, s59, s51\n\t"
"s_lshl_b32 s60, s49, 1\n\t"
"s_add_u32 s59, s59, s60\n\t"
"s_lshl_b32 s59, s59, 2\n\t"
"s_add_u32 s59, s59, s48\n\t"
"s_lshl_b32 s59, s59, 16\n\t"
"s_and_b32 s60, s45, 1\n\t"
"s_lshl_b32 s60, s60, 13\n\t"
"s_add_u32 s59, s59, s60\n\t"
"s_add_u32 s68, s59, 16777216\n\t"
"s_lshl_b32 s59, s50, 18\n\t"
"s_lshl_b32 s60, s52, 13\n\t"
"s_add_u32 s59, s59, s60\n\t"
"s_add_u32 s69, s59, 8388608\n\t"
"s_lshl_b32 s59, s48, 3\n\t"
"s_add_u32 s59, s59, s50\n\t"
"s_lshl_b32 s59, s59, 16\n\t"
"s_add_u32 s70, s59, 41943040\n\t"
"v_add_u32 v238, s64, v22\n\t"
"v_add_u32 v236, 65536, v22\n\t"
"s_add_u32 s59, s62, 32768\n\t"
"v_add_u32 v237, s59, v22\n\t"
"v_mov_b32 v12, v22\n\t"
"s_waitcnt vmcnt(12)\n\t"
"s_barrier\n\t"
"s_add_u32 m0, s62, 0\n\t"
"s_nop 0\n\t"
"buffer_load_dwordx4 v0, s[28:31], s65 offen lds\n\t"
"buffer_load_dwordx4 v1, s[28:31], s65 offen offset:1024 lds\n\t"
"buffer_load_dwordx4 v2, s[28:31], s65 offen offset:2048 lds\n\t"
"buffer_load_dwordx4 v3, s[28:31], s65 offen offset:3072 lds\n\t"
"s_add_u32 m0, m0, 4096\n\t"
"s_nop 0\n\t"
"buffer_load_dwordx4 v4, s[28:31], s65 offen lds\n\t"
"buffer_load_dwordx4 v5, s[28:31], s65 offen offset:1024 lds\n\t"
"buffer_load_dwordx4 v6, s[28:31], s65 offen offset:2048 lds\n\t"
"buffer_load_dwordx4 v7, s[28:31], s65 offen offset:3072 lds\n\t"
"s_add_u32 s65, s65, 128\n\t"
"s_add_u32 m0, s63, 131072\n\t"
"s_nop 0\n\t"
"buffer_load_dwordx4 v8, s[24:27], s77 offen sc1 lds\n\t"
"buffer_load_dwordx4 v8, s[24:27], s77 offen offset:1024 sc1 lds\n\t"
"buffer_load_dwordx4 v8, s[24:27], s77 offen offset:2048 sc1 lds\n\t"
"buffer_load_dwordx4 v8, s[24:27], s77 offen offset:3072 sc1 lds\n\t"
"s_add_u32 s77, s77, 16384\n\t"
"s_add_u32 m0, s63, 114688\n\t"
"s_mov_b32 s59, 3840\n\t"
"buffer_load_dwordx4 v244, s[20:23], s59 offen lds\n\t"
"buffer_load_dwordx4 v245, s[20:23], s59 offen offset:1024 lds\n\t"
"buffer_load_dwordx4 v246, s[20:23], s59 offen offset:2048 lds\n\t"
"buffer_load_dwordx4 v247, s[20:23], s59 offen offset:3072 lds\n\t"
"s_waitcnt vmcnt(16)\n\t"
"s_barrier\n\t"
"s_add_u32 m0, s62, 32768\n\t"
"s_nop 0\n\t"
"buffer_load_dwordx4 v0, s[28:31], s65 offen lds\n\t"
"buffer_load_dwordx4 v1, s[28:31], s65 offen offset:1024 lds\n\t"
"buffer_load_dwordx4 v2, s[28:31], s65 offen offset:2048 lds\n\t"
"buffer_load_dwordx4 v3, s[28:31], s65 offen offset:3072 lds\n\t"
"s_add_u32 m0, m0, 4096\n\t"
"s_nop 0\n\t"
"buffer_load_dwordx4 v4, s[28:31], s65 offen lds\n\t"
"buffer_load_dwordx4 v5, s[28:31], s65 offen offset:1024 lds\n\t"
"buffer_load_dwordx4 v6, s[28:31], s65 offen offset:2048 lds\n\t"
"buffer_load_dwordx4 v7, s[28:31], s65 offen offset:3072 lds\n\t"
"s_add_u32 s65, s65, 128\n\t"
"s_add_u32 m0, s63, 147456\n\t"
"s_nop 0\n\t"
"buffer_load_dwordx4 v8, s[24:27], s77 offen sc1 lds\n\t"
"buffer_load_dwordx4 v8, s[24:27], s77 offen offset:1024 sc1 lds\n\t"
"buffer_load_dwordx4 v8, s[24:27], s77 offen offset:2048 sc1 lds\n\t"
"buffer_load_dwordx4 v8, s[24:27], s77 offen offset:3072 sc1 lds\n\t"
"s_add_u32 s77, s77, 16384\n\t"
"s_waitcnt vmcnt(12)\n\t"
"s_barrier\n\t"
"s_add_u32 m0, s62, 65536\n\t"
"s_nop 0\n\t"
"buffer_load_dwordx4 v0, s[28:31], s65 offen lds\n\t"
"buffer_load_dwordx4 v1, s[28:31], s65 offen offset:1024 lds\n\t"
"buffer_load_dwordx4 v2, s[28:31], s65 offen offset:2048 lds\n\t"
"buffer_load_dwordx4 v3, s[28:31], s65 offen offset:3072 lds\n\t"
"s_add_u32 m0, m0, 4096\n\t"
"s_nop 0\n\t"
"buffer_load_dwordx4 v4, s[28:31], s65 offen lds\n\t"
"buffer_load_dwordx4 v5, s[28:31], s65 offen offset:1024 lds\n\t"
"buffer_load_dwordx4 v6, s[28:31], s65 offen offset:2048 lds\n\t"
"buffer_load_dwordx4 v7, s[28:31], s65 offen offset:3072 lds\n\t"
"s_add_u32 s65, s65, 128\n\t"
"s_add_u32 m0, s63, 98304\n\t"
"s_nop 0\n\t"
"buffer_load_dwordx4 v8, s[24:27], s77 offen sc1 lds\n\t"
"buffer_load_dwordx4 v8, s[24:27], s77 offen offset:1024 sc1 lds\n\t"
"buffer_load_dwordx4 v8, s[24:27], s77 offen offset:2048 sc1 lds\n\t"
"buffer_load_dwordx4 v8, s[24:27], s77 offen offset:3072 sc1 lds\n\t"
"s_add_u32 s77, s77, 16384\n\t"
"s_lshl_b32 s59, s46, 5\n\t"
"s_add_u32 s59, s59, s47\n\t"
"s_lshl_b32 s59, s59, 7\n\t"
"s_add_u32 s59, s59, 33619968\n\t"
"s_cmp_lg_u32 s45, 0\n\t"
"s_cbranch_scc1 L_nopub2_%=\n\t"
"v_mov_b32 v14, s53\n\t"
"v_mov_b32 v15, 0\n\t"
"s_mov_b64 exec, 1\n\t"
"buffer_store_dword v14, v15, s[24:27], s59 offen sc1\n\t"
"s_mov_b64 exec, -1\n\t"
"L_nopub2_%=:\n\t"
"ds_read_b128 v[224:227], v248 offset:0\n\t"
"ds_read_b128 v[228:231], v248 offset:2048\n\t"
"ds_read_b128 v[232:235], v249 offset:0\n\t"
"ds_read_b128 v[240:243], v249 offset:2048\n\t"
"s_waitcnt lgkmcnt(0)\n\t"
"s_add_u32 m0, s63, 114688\n\t"
"s_mov_b32 s59, 3968\n\t"
"buffer_load_dwordx4 v244, s[20:23], s59 offen lds\n\t"
"buffer_load_dwordx4 v245, s[20:23], s59 offen offset:1024 lds\n\t"
"buffer_load_dwordx4 v246, s[20:23], s59 offen offset:2048 lds\n\t"
"buffer_load_dwordx4 v247, s[20:23], s59 offen offset:3072 lds\n\t"
"s_waitcnt vmcnt(16)\n\t"
"s_barrier\n\t"
"s_add_u32 m0, s62, 0\n\t"
"s_nop 0\n\t"
"buffer_load_dwordx4 v0, s[28:31], s65 offen lds\n\t"
"buffer_load_dwordx4 v1, s[28:31], s65 offen offset:1024 lds\n\t"
"buffer_load_dwordx4 v2, s[28:31], s65 offen offset:2048 lds\n\t"
"buffer_load_dwordx4 v3, s[28:31], s65 offen offset:3072 lds\n\t"
"s_add_u32 m0, m0, 4096\n\t"
"s_nop 0\n\t"
"buffer_load_dwordx4 v4, s[28:31], s65 offen lds\n\t"
"buffer_load_dwordx4 v5, s[28:31], s65 offen offset:1024 lds\n\t"
"buffer_load_dwordx4 v6, s[28:31], s65 offen offset:2048 lds\n\t"
"buffer_load_dwordx4 v7, s[28:31], s65 offen offset:3072 lds\n\t"
"s_add_u32 s65, s65, 128\n\t"
"s_add_u32 m0, s63, 131072\n\t"
"s_nop 0\n\t"
"buffer_load_dwordx4 v8, s[24:27], s77 offen sc1 lds\n\t"
"buffer_load_dwordx4 v8, s[24:27], s77 offen offset:1024 sc1 lds\n\t"
"buffer_load_dwordx4 v8, s[24:27], s77 offen offset:2048 sc1 lds\n\t"
"buffer_load_dwordx4 v8, s[24:27], s77 offen offset:3072 sc1 lds\n\t"
"s_add_u32 s77, s77, 16384\n\t"
"s_waitcnt vmcnt(16)\n\t"
"s_barrier\n\t"
"s_add_u32 m0, s62, 32768\n\t"
"s_nop 0\n\t"
"buffer_load_dwordx4 v0, s[28:31], s65 offen lds\n\t"
"buffer_load_dwordx4 v1, s[28:31], s65 offen offset:1024 lds\n\t"
"buffer_load_dwordx4 v2, s[28:31], s65 offen offset:2048 lds\n\t"
"buffer_load_dwordx4 v3, s[28:31], s65 offen offset:3072 lds\n\t"
"s_add_u32 m0, m0, 4096\n\t"
"s_nop 0\n\t"
"buffer_load_dwordx4 v4, s[28:31], s65 offen lds\n\t"
"buffer_load_dwordx4 v5, s[28:31], s65 offen offset:1024 lds\n\t"
"buffer_load_dwordx4 v6, s[28:31], s65 offen offset:2048 lds\n\t"
"buffer_load_dwordx4 v7, s[28:31], s65 offen offset:3072 lds\n\t"
"s_add_u32 s65, s65, 128\n\t"
"s_add_u32 m0, s63, 147456\n\t"
"s_nop 0\n\t"
"buffer_load_dwordx4 v8, s[24:27], s77 offen sc1 lds\n\t"
"buffer_load_dwordx4 v8, s[24:27], s77 offen offset:1024 sc1 lds\n\t"
"buffer_load_dwordx4 v8, s[24:27], s77 offen offset:2048 sc1 lds\n\t"
"buffer_load_dwordx4 v8, s[24:27], s77 offen offset:3072 sc1 lds\n\t"
"s_add_u32 s77, s77, 16384\n\t"
"s_waitcnt vmcnt(12)\n\t"
"s_barrier\n\t"
"s_add_u32 m0, s62, 65536\n\t"
"s_nop 0\n\t"
"buffer_load_dwordx4 v0, s[28:31], s65 offen lds\n\t"
"buffer_load_dwordx4 v1, s[28:31], s65 offen offset:1024 lds\n\t"
"buffer_load_dwordx4 v2, s[28:31], s65 offen offset:2048 lds\n\t"
"buffer_load_dwordx4 v3, s[28:31], s65 offen offset:3072 lds\n\t"
"s_add_u32 m0, m0, 4096\n\t"
"s_nop 0\n\t"
"buffer_load_dwordx4 v4, s[28:31], s65 offen lds\n\t"
"buffer_load_dwordx4 v5, s[28:31], s65 offen offset:1024 lds\n\t"
"buffer_load_dwordx4 v6, s[28:31], s65 offen offset:2048 lds\n\t"
"buffer_load_dwordx4 v7, s[28:31], s65 offen offset:3072 lds\n\t"
"s_add_u32 s65, s65, 128\n\t"
"s_add_u32 m0, s63, 98304\n\t"
"s_nop 0\n\t"
"buffer_load_dwordx4 v8, s[24:27], s77 offen sc1 lds\n\t"
"buffer_load_dwordx4 v8, s[24:27], s77 offen offset:1024 sc1 lds\n\t"
"buffer_load_dwordx4 v8, s[24:27], s77 offen offset:2048 sc1 lds\n\t"
"buffer_load_dwordx4 v8, s[24:27], s77 offen offset:3072 sc1 lds\n\t"
"s_add_u32 s77, s77, 16384\n\t"
"s_waitcnt vmcnt(12)\n\t"
"s_barrier\n\t"
"s_add_u32 m0, s62, 0\n\t"
"s_nop 0\n\t"
"buffer_load_dwordx4 v0, s[28:31], s65 offen lds\n\t"
"buffer_load_dwordx4 v1, s[28:31], s65 offen offset:1024 lds\n\t"
"buffer_load_dwordx4 v2, s[28:31], s65 offen offset:2048 lds\n\t"
"buffer_load_dwordx4 v3, s[28:31], s65 offen offset:3072 lds\n\t"
"s_add_u32 m0, m0, 4096\n\t"
"s_nop 0\n\t"
"buffer_load_dwordx4 v4, s[28:31], s65 offen lds\n\t"
"buffer_load_dwordx4 v5, s[28:31], s65 offen offset:1024 lds\n\t"
"buffer_load_dwordx4 v6, s[28:31], s65 offen offset:2048 lds\n\t"
"buffer_load_dwordx4 v7, s[28:31], s65 offen offset:3072 lds\n\t"
"s_add_u32 s65, s65, 128\n\t"
"s_add_u32 m0, s63, 131072\n\t"
"s_nop 0\n\t"
"buffer_load_dwordx4 v8, s[24:27], s77 offen sc1 lds\n\t"
"buffer_load_dwordx4 v8, s[24:27], s77 offen offset:1024 sc1 lds\n\t"
"buffer_load_dwordx4 v8, s[24:27], s77 offen offset:2048 sc1 lds\n\t"
"buffer_load_dwordx4 v8, s[24:27], s77 offen offset:3072 sc1 lds\n\t"
"s_add_u32 s77, s77, 16384\n\t"
"s_waitcnt vmcnt(12)\n\t"
"s_barrier\n\t"
"s_add_u32 m0, s62, 32768\n\t"
"s_nop 0\n\t"
"buffer_load_dwordx4 v0, s[28:31], s65 offen lds\n\t"
"buffer_load_dwordx4 v1, s[28:31], s65 offen offset:1024 lds\n\t"
"buffer_load_dwordx4 v2, s[28:31], s65 offen offset:2048 lds\n\t"
"buffer_load_dwordx4 v3, s[28:31], s65 offen offset:3072 lds\n\t"
"s_add_u32 m0, m0, 4096\n\t"
"s_nop 0\n\t"
"buffer_load_dwordx4 v4, s[28:31], s65 offen lds\n\t"
"buffer_load_dwordx4 v5, s[28:31], s65 offen offset:1024 lds\n\t"
"buffer_load_dwordx4 v6, s[28:31], s65 offen offset:2048 lds\n\t"
"buffer_load_dwordx4 v7, s[28:31], s65 offen offset:3072 lds\n\t"
"s_add_u32 s65, s65, 128\n\t"
"s_add_u32 m0, s63, 147456\n\t"
"s_nop 0\n\t"
"buffer_load_dwordx4 v8, s[24:27], s77 offen sc1 lds\n\t"
"buffer_load_dwordx4 v8, s[24:27], s77 offen offset:1024 sc1 lds\n\t"
"buffer_load_dwordx4 v8, s[24:27], s77 offen offset:2048 sc1 lds\n\t"
"buffer_load_dwordx4 v8, s[24:27], s77 offen offset:3072 sc1 lds\n\t"
"s_add_u32 s77, s77, 16384\n\t"
"s_waitcnt vmcnt(12)\n\t"
"s_barrier\n\t"
"s_add_u32 m0, s62, 65536\n\t"
"s_nop 0\n\t"
"buffer_load_dwordx4 v0, s[28:31], s65 offen lds\n\t"
"buffer_load_dwordx4 v1, s[28:31], s65 offen offset:1024 lds\n\t"
"buffer_load_dwordx4 v2, s[28:31], s65 offen offset:2048 lds\n\t"
"buffer_load_dwordx4 v3, s[28:31], s65 offen offset:3072 lds\n\t"
"s_add_u32 m0, m0, 4096\n\t"
"s_nop 0\n\t"
"buffer_load_dwordx4 v4, s[28:31], s65 offen lds\n\t"
"buffer_load_dwordx4 v5, s[28:31], s65 offen offset:1024 lds\n\t"
"buffer_load_dwordx4 v6, s[28:31], s65 offen offset:2048 lds\n\t"
"buffer_load_dwordx4 v7, s[28:31], s65 offen offset:3072 lds\n\t"
"s_add_u32 s65, s65, 128\n\t"
"s_add_u32 m0, s63, 98304\n\t"
"s_nop 0\n\t"
"buffer_load_dwordx4 v8, s[24:27], s77 offen sc1 lds\n\t"
"buffer_load_dwordx4 v8, s[24:27], s77 offen offset:1024 sc1 lds\n\t"
"buffer_load_dwordx4 v8, s[24:27], s77 offen offset:2048 sc1 lds\n\t"
"buffer_load_dwordx4 v8, s[24:27], s77 offen offset:3072 sc1 lds\n\t"
"s_add_u32 s77, s77, 16384\n\t"
"s_waitcnt vmcnt(12)\n\t"
"s_barrier\n\t"
"buffer_load_dword v14, v13, s[24:27], 0 offen sc1\n\t"
"s_add_u32 m0, s62, 0\n\t"
"s_nop 0\n\t"
"buffer_load_dwordx4 v0, s[28:31], s65 offen lds\n\t"
"buffer_load_dwordx4 v1, s[28:31], s65 offen offset:1024 lds\n\t"
"buffer_load_dwordx4 v2, s[28:31], s65 offen offset:2048 lds\n\t"
"buffer_load_dwordx4 v3, s[28:31], s65 offen offset:3072 lds\n\t"
"s_add_u32 m0, m0, 4096\n\t"
"s_nop 0\n\t"
"buffer_load_dwordx4 v4, s[28:31], s65 offen lds\n\t"
"buffer_load_dwordx4 v5, s[28:31], s65 offen offset:1024 lds\n\t"
"buffer_load_dwordx4 v6, s[28:31], s65 offen offset:2048 lds\n\t"
"buffer_load_dwordx4 v7, s[28:31], s65 offen offset:3072 lds\n\t"
"s_add_u32 s65, s65, 128\n\t"
"s_add_u32 m0, s63, 131072\n\t"
"s_nop 0\n\t"
"buffer_load_dwordx4 v8, s[24:27], s77 offen sc1 lds\n\t"
"buffer_load_dwordx4 v8, s[24:27], s77 offen offset:1024 sc1 lds\n\t"
"buffer_load_dwordx4 v8, s[24:27], s77 offen offset:2048 sc1 lds\n\t"
"buffer_load_dwordx4 v8, s[24:27], s77 offen offset:3072 sc1 lds\n\t"
"s_add_u32 s77, s77, 16384\n\t"
"s_waitcnt vmcnt(12)\n\t"
"s_barrier\n\t"
"v_cmp_ne_u32 vcc, s53, v14\n\t"
"s_nop 4\n\t"
"s_cbranch_vccz L_f2ok_%=\n\t"
"s_mov_b32 s78, 0x400000\n\t"
"L_pf2_%=:\n\t"
"buffer_load_dword v14, v13, s[24:27], 0 offen sc1\n\t"
"s_waitcnt vmcnt(0)\n\t"
"v_cmp_ne_u32 vcc, s53, v14\n\t"
"s_nop 4\n\t"
"s_cbranch_vccz L_f2ok_%=\n\t"
"s_sleep 2\n\t"
"s_sub_u32 s78, s78, 1\n\t"
"s_cmp_lg_u32 s78, 0\n\t"
"s_cbranch_scc1 L_pf2_%=\n\t"
"L_f2ok_%=:\n\t"
"s_mov_b32 s77, s70\n\t"
"s_add_u32 m0, s62, 32768\n\t"
"s_add_u32 s59, s68, 0\n\t"
"buffer_load_dwordx4 v12, s[24:27], s59 offen sc1 lds\n\t"
"buffer_load_dwordx4 v12, s[24:27], s59 offen offset:1024 sc1 lds\n\t"
"buffer_load_dwordx4 v12, s[24:27], s59 offen offset:2048 sc1 lds\n\t"
"buffer_load_dwordx4 v12, s[24:27], s59 offen offset:3072 sc1 lds\n\t"
"s_add_u32 m0, m0, 4096\n\t"
"s_add_u32 s59, s59, 4096\n\t"
"buffer_load_dwordx4 v12, s[24:27], s59 offen sc1 lds\n\t"
"buffer_load_dwordx4 v12, s[24:27], s59 offen offset:1024 sc1 lds\n\t"
"buffer_load_dwordx4 v12, s[24:27], s59 offen offset:2048 sc1 lds\n\t"
"buffer_load_dwordx4 v12, s[24:27], s59 offen offset:3072 sc1 lds\n\t"
"s_add_u32 m0, s63, 147456\n\t"
"s_nop 0\n\t"
"buffer_load_dwordx4 v8, s[24:27], s77 offen sc1 lds\n\t"
"buffer_load_dwordx4 v8, s[24:27], s77 offen offset:1024 sc1 lds\n\t"
"buffer_load_dwordx4 v8, s[24:27], s77 offen offset:2048 sc1 lds\n\t"
"buffer_load_dwordx4 v8, s[24:27], s77 offen offset:3072 sc1 lds\n\t"
"s_add_u32 s77, s77, 16384\n\t"
"s_waitcnt vmcnt(12)\n\t"
"s_barrier\n\t"
"s_add_u32 m0, s62, 65536\n\t"
"s_add_u32 s59, s68, 16384\n\t"
"buffer_load_dwordx4 v12, s[24:27], s59 offen sc1 lds\n\t"
"buffer_load_dwordx4 v12, s[24:27], s59 offen offset:1024 sc1 lds\n\t"
"buffer_load_dwordx4 v12, s[24:27], s59 offen offset:2048 sc1 lds\n\t"
"buffer_load_dwordx4 v12, s[24:27], s59 offen offset:3072 sc1 lds\n\t"
"s_add_u32 m0, m0, 4096\n\t"
"s_add_u32 s59, s59, 4096\n\t"
"buffer_load_dwordx4 v12, s[24:27], s59 offen sc1 lds\n\t"
"buffer_load_dwordx4 v12, s[24:27], s59 offen offset:1024 sc1 lds\n\t"
"buffer_load_dwordx4 v12, s[24:27], s59 offen offset:2048 sc1 lds\n\t"
"buffer_load_dwordx4 v12, s[24:27], s59 offen offset:3072 sc1 lds\n\t"
"s_add_u32 m0, s63, 98304\n\t"
"s_nop 0\n\t"
"buffer_load_dwordx4 v8, s[24:27], s77 offen sc1 lds\n\t"
"buffer_load_dwordx4 v8, s[24:27], s77 offen offset:1024 sc1 lds\n\t"
"buffer_load_dwordx4 v8, s[24:27], s77 offen offset:2048 sc1 lds\n\t"
"buffer_load_dwordx4 v8, s[24:27], s77 offen offset:3072 sc1 lds\n\t"
"s_add_u32 s77, s77, 16384\n\t"
"s_waitcnt vmcnt(12)\n\t"
"s_barrier\n\t"
"s_add_u32 m0, s62, 0\n\t"
"s_add_u32 s59, s68, 32768\n\t"
"buffer_load_dwordx4 v12, s[24:27], s59 offen sc1 lds\n\t"
"buffer_load_dwordx4 v12, s[24:27], s59 offen offset:1024 sc1 lds\n\t"
"buffer_load_dwordx4 v12, s[24:27], s59 offen offset:2048 sc1 lds\n\t"
"buffer_load_dwordx4 v12, s[24:27], s59 offen offset:3072 sc1 lds\n\t"
"s_add_u32 m0, m0, 4096\n\t"
"s_add_u32 s59, s59, 4096\n\t"
"buffer_load_dwordx4 v12, s[24:27], s59 offen sc1 lds\n\t"
"buffer_load_dwordx4 v12, s[24:27], s59 offen offset:1024 sc1 lds\n\t"
"buffer_load_dwordx4 v12, s[24:27], s59 offen offset:2048 sc1 lds\n\t"
"buffer_load_dwordx4 v12, s[24:27], s59 offen offset:3072 sc1 lds\n\t"
"s_add_u32 m0, s63, 131072\n\t"
"s_nop 0\n\t"
"buffer_load_dwordx4 v8, s[24:27], s77 offen sc1 lds\n\t"
"buffer_load_dwordx4 v8, s[24:27], s77 offen offset:1024 sc1 lds\n\t"
"buffer_load_dwordx4 v8, s[24:27], s77 offen offset:2048 sc1 lds\n\t"
"buffer_load_dwordx4 v8, s[24:27], s77 offen offset:3072 sc1 lds\n\t"
"s_add_u32 s77, s77, 16384\n\t"
"s_waitcnt vmcnt(12)\n\t"
"s_barrier\n\t"
"s_add_u32 m0, s62, 32768\n\t"
"s_add_u32 s59, s68, 49152\n\t"
"buffer_load_dwordx4 v12, s[24:27], s59 offen sc1 lds\n\t"
"buffer_load_dwordx4 v12, s[24:27], s59 offen offset:1024 sc1 lds\n\t"
"buffer_load_dwordx4 v12, s[24:27], s59 offen offset:2048 sc1 lds\n\t"
"buffer_load_dwordx4 v12, s[24:27], s59 offen offset:3072 sc1 lds\n\t"
"s_add_u32 m0, m0, 4096\n\t"
"s_add_u32 s59, s59, 4096\n\t"
"buffer_load_dwordx4 v12, s[24:27], s59 offen sc1 lds\n\t"
"buffer_load_dwordx4 v12, s[24:27], s59 offen offset:1024 sc1 lds\n\t"
"buffer_load_dwordx4 v12, s[24:27], s59 offen offset:2048 sc1 lds\n\t"
"buffer_load_dwordx4 v12, s[24:27], s59 offen offset:3072 sc1 lds\n\t"
"s_add_u32 m0, s63, 147456\n\t"
"s_nop 0\n\t"
"buffer_load_dwordx4 v8, s[24:27], s77 offen sc1 lds\n\t"
"buffer_load_dwordx4 v8, s[24:27], s77 offen offset:1024 sc1 lds\n\t"
"buffer_load_dwordx4 v8, s[24:27], s77 offen offset:2048 sc1 lds\n\t"
"buffer_load_dwordx4 v8, s[24:27], s77 offen offset:3072 sc1 lds\n\t"
"s_add_u32 s77, s77, 16384\n\t"
"s_waitcnt vmcnt(12)\n\t"
"s_barrier\n\t"
"s_add_u32 m0, s64, 65536\n\t"
"s_add_u32 s59, s69, 212992\n\t"
"buffer_load_dwordx4 v238, s[24:27], s59 offen sc1 lds\n\t"
"buffer_load_dwordx4 v238, s[24:27], s59 offen offset:1024 sc1 lds\n\t"
"s_add_u32 m0, s64, 73728\n\t"
"s_add_u32 s59, s69, 229376\n\t"
"buffer_load_dwordx4 v238, s[24:27], s59 offen sc1 lds\n\t"
"buffer_load_dwordx4 v238, s[24:27], s59 offen offset:1024 sc1 lds\n\t"
"s_add_u32 m0, s64, 81920\n\t"
"s_add_u32 s59, s69, 245760\n\t"
"buffer_load_dwordx4 v238, s[24:27], s59 offen sc1 lds\n\t"
"buffer_load_dwordx4 v238, s[24:27], s59 offen offset:1024 sc1 lds\n\t"
"s_add_u32 m0, s64, 90112\n\t"
"s_add_u32 s59, s69, 2310144\n\t"
"buffer_load_dwordx4 v238, s[24:27], s59 offen sc1 lds\n\t"
"buffer_load_dwordx4 v238, s[24:27], s59 offen offset:1024 sc1 lds\n\t"
"s_add_u32 m0, s64, 98304\n\t"
"s_add_u32 s59, s69, 2326528\n\t"
"buffer_load_dwordx4 v238, s[24:27], s59 offen sc1 lds\n\t"
"buffer_load_dwordx4 v238, s[24:27], s59 offen offset:1024 sc1 lds\n\t"
"s_add_u32 m0, s64, 106496\n\t"
"s_add_u32 s59, s69, 2342912\n\t"
"buffer_load_dwordx4 v238, s[24:27], s59 offen sc1 lds\n\t"
"buffer_load_dwordx4 v238, s[24:27], s59 offen offset:1024 sc1 lds\n\t"
"s_waitcnt vmcnt(12)\n\t"
"s_barrier\n\t"
"s_add_u32 m0, s64, 0\n\t"
"s_add_u32 s59, s69, 4407296\n\t"
"buffer_load_dwordx4 v238, s[24:27], s59 offen sc1 lds\n\t"
"buffer_load_dwordx4 v238, s[24:27], s59 offen offset:1024 sc1 lds\n\t"
"s_add_u32 m0, s64, 8192\n\t"
"s_add_u32 s59, s69, 4423680\n\t"
"buffer_load_dwordx4 v238, s[24:27], s59 offen sc1 lds\n\t"
"buffer_load_dwordx4 v238, s[24:27], s59 offen offset:1024 sc1 lds\n\t"
"s_add_u32 m0, s64, 16384\n\t"
"s_add_u32 s59, s69, 4440064\n\t"
"buffer_load_dwordx4 v238, s[24:27], s59 offen sc1 lds\n\t"
"buffer_load_dwordx4 v238, s[24:27], s59 offen offset:1024 sc1 lds\n\t"
"s_add_u32 m0, s64, 24576\n\t"
"s_add_u32 s59, s69, 6504448\n\t"
"buffer_load_dwordx4 v238, s[24:27], s59 offen sc1 lds\n\t"
"buffer_load_dwordx4 v238, s[24:27], s59 offen offset:1024 sc1 lds\n\t"
"s_add_u32 m0, s64, 131072\n\t"
"s_add_u32 s59, s69, 6520832\n\t"
"buffer_load_dwordx4 v238, s[24:27], s59 offen sc1 lds\n\t"
"buffer_load_dwordx4 v238, s[24:27], s59 offen offset:1024 sc1 lds\n\t"
"s_add_u32 m0, s64, 139264\n\t"
"s_add_u32 s59, s69, 6537216\n\t"
"buffer_load_dwordx4 v238, s[24:27], s59 offen sc1 lds\n\t"
"buffer_load_dwordx4 v238, s[24:27], s59 offen offset:1024 sc1 lds\n\t"
"s_waitcnt vmcnt(0)\n\t"
"s_barrier\n\t"
"ds_read_b128 v[0:3], v236 offset:0\n\t"
"ds_read_b128 v[4:7], v236 offset:2048\n\t"
"ds_read_b128 v[8:11], v236 offset:4096\n\t"
"ds_read_b128 v[12:15], v236 offset:6144\n\t"
"ds_read_b128 v[16:19], v236 offset:1024\n\t"
"ds_read_b128 v[20:23], v236 offset:3072\n\t"
"ds_read_b128 v[24:27], v236 offset:5120\n\t"
"ds_read_b128 v[28:31], v236 offset:7168\n\t"
"s_waitcnt lgkmcnt(7)\n\t"
"v_mfma_f32_16x16x32_bf16 v[32:35], v[0:3], v[64:67], 0\n\t"
"v_mfma_f32_16x16x32_bf16 v[48:51], v[0:3], v[68:71], 0\n\t"
"s_waitcnt lgkmcnt(6)\n\t"
"v_mfma_f32_16x16x32_bf16 v[52:55], v[4:7], v[68:71], 0\n\t"
"ds_read_b128 v[0:3], v236 offset:8192\n\t"
"v_mfma_f32_16x16x32_bf16 v[36:39], v[4:7], v[64:67], 0\n\t"
"s_waitcnt lgkmcnt(6)\n\t"
"v_mfma_f32_16x16x32_bf16 v[40:43], v[8:11], v[64:67], 0\n\t"
"ds_read_b128 v[4:7], v236 offset:10240\n\t"
"v_mfma_f32_16x16x32_bf16 v[56:59], v[8:11], v[68:71], 0\n\t"
"s_waitcnt lgkmcnt(6)\n\t"
"v_mfma_f32_16x16x32_bf16 v[60:63], v[12:15], v[68:71], 0\n\t"
"ds_read_b128 v[8:11], v236 offset:12288\n\t"
"v_mfma_f32_16x16x32_bf16 v[44:47], v[12:15], v[64:67], 0\n\t"
"s_waitcnt lgkmcnt(6)\n\t"
"v_mfma_f32_16x16x32_bf16 v[32:35], v[16:19], v[72:75], v[32:35]\n\t"
"ds_read_b128 v[12:15], v236 offset:14336\n\t"
"v_mfma_f32_16x16x32_bf16 v[48:51], v[16:19], v[76:79], v[48:51]\n\t"
"s_waitcnt lgkmcnt(6)\n\t"
"v_mfma_f32_16x16x32_bf16 v[52:55], v[20:23], v[76:79], v[52:55]\n\t"
"ds_read_b128 v[16:19], v236 offset:9216\n\t"
"v_mfma_f32_16x16x32_bf16 v[36:39], v[20:23], v[72:75], v[36:39]\n\t"
"s_waitcnt lgkmcnt(6)\n\t"
"v_mfma_f32_16x16x32_bf16 v[40:43], v[24:27], v[72:75], v[40:43]\n\t"
"ds_read_b128 v[20:23], v236 offset:11264\n\t"
"v_mfma_f32_16x16x32_bf16 v[56:59], v[24:27], v[76:79], v[56:59]\n\t"
"s_waitcnt lgkmcnt(6)\n\t"
"v_mfma_f32_16x16x32_bf16 v[60:63], v[28:31], v[76:79], v[60:63]\n\t"
"ds_read_b128 v[24:27], v236 offset:13312\n\t"
"v_mfma_f32_16x16x32_bf16 v[44:47], v[28:31], v[72:75], v[44:47]\n\t"
"ds_read_b128 v[28:31], v236 offset:15360\n\t"
"s_waitcnt lgkmcnt(7)\n\t"
"v_mfma_f32_16x16x32_bf16 v[32:35], v[0:3], v[80:83], v[32:35]\n\t"
"v_mfma_f32_16x16x32_bf16 v[48:51], v[0:3], v[84:87], v[48:51]\n\t"
"s_waitcnt lgkmcnt(6)\n\t"
"v_mfma_f32_16x16x32_bf16 v[52:55], v[4:7], v[84:87], v[52:55]\n\t"
"ds_read_b128 v[0:3], v236 offset:16384\n\t"
"v_mfma_f32_16x16x32_bf16 v[36:39], v[4:7], v[80:83], v[36:39]\n\t"
"s_waitcnt lgkmcnt(6)\n\t"
"v_mfma_f32_16x16x32_bf16 v[40:43], v[8:11], v[80:83], v[40:43]\n\t"
"ds_read_b128 v[4:7], v236 offset:18432\n\t"
"v_mfma_f32_16x16x32_bf16 v[56:59], v[8:11], v[84:87], v[56:59]\n\t"
"s_waitcnt lgkmcnt(6)\n\t"
"v_mfma_f32_16x16x32_bf16 v[60:63], v[12:15], v[84:87], v[60:63]\n\t"
"ds_read_b128 v[8:11], v236 offset:20480\n\t"
"v_mfma_f32_16x16x32_bf16 v[44:47], v[12:15], v[80:83], v[44:47]\n\t"
"s_waitcnt lgkmcnt(6)\n\t"
"v_mfma_f32_16x16x32_bf16 v[32:35], v[16:19], v[88:91], v[32:35]\n\t"
"ds_read_b128 v[12:15], v236 offset:22528\n\t"
"v_mfma_f32_16x16x32_bf16 v[48:51], v[16:19], v[92:95], v[48:51]\n\t"
"s_waitcnt lgkmcnt(6)\n\t"
"v_mfma_f32_16x16x32_bf16 v[52:55], v[20:23], v[92:95], v[52:55]\n\t"
"ds_read_b128 v[16:19], v236 offset:17408\n\t"
"v_mfma_f32_16x16x32_bf16 v[36:39], v[20:23], v[88:91], v[36:39]\n\t"
"s_waitcnt lgkmcnt(6)\n\t"
"v_mfma_f32_16x16x32_bf16 v[40:43], v[24:27], v[88:91], v[40:43]\n\t"
"ds_read_b128 v[20:23], v236 offset:19456\n\t"
"v_mfma_f32_16x16x32_bf16 v[56:59], v[24:27], v[92:95], v[56:59]\n\t"
"s_waitcnt lgkmcnt(6)\n\t"
"v_mfma_f32_16x16x32_bf16 v[60:63], v[28:31], v[92:95], v[60:63]\n\t"
"ds_read_b128 v[24:27], v236 offset:21504\n\t"
"v_mfma_f32_16x16x32_bf16 v[44:47], v[28:31], v[88:91], v[44:47]\n\t"
"ds_read_b128 v[28:31], v236 offset:23552\n\t"
"s_waitcnt lgkmcnt(7)\n\t"
"v_mfma_f32_16x16x32_bf16 v[32:35], v[0:3], v[96:99], v[32:35]\n\t"
"v_mfma_f32_16x16x32_bf16 v[48:51], v[0:3], v[100:103], v[48:51]\n\t"
"s_waitcnt lgkmcnt(6)\n\t"
"v_mfma_f32_16x16x32_bf16 v[52:55], v[4:7], v[100:103], v[52:55]\n\t"
"ds_read_b128 v[0:3], v236 offset:24576\n\t"
"v_mfma_f32_16x16x32_bf16 v[36:39], v[4:7], v[96:99], v[36:39]\n\t"
"s_waitcnt lgkmcnt(6)\n\t"
"v_mfma_f32_16x16x32_bf16 v[40:43], v[8:11], v[96:99], v[40:43]\n\t"
"ds_read_b128 v[4:7], v236 offset:26624\n\t"
"v_mfma_f32_16x16x32_bf16 v[56:59], v[8:11], v[100:103], v[56:59]\n\t"
"s_waitcnt lgkmcnt(6)\n\t"
"v_mfma_f32_16x16x32_bf16 v[60:63], v[12:15], v[100:103], v[60:63]\n\t"
"ds_read_b128 v[8:11], v236 offset:28672\n\t"
"v_mfma_f32_16x16x32_bf16 v[44:47], v[12:15], v[96:99], v[44:47]\n\t"
"s_waitcnt lgkmcnt(6)\n\t"
"v_mfma_f32_16x16x32_bf16 v[32:35], v[16:19], v[104:107], v[32:35]\n\t"
"ds_read_b128 v[12:15], v236 offset:30720\n\t"
"v_mfma_f32_16x16x32_bf16 v[48:51], v[16:19], v[108:111], v[48:51]\n\t"
"s_waitcnt lgkmcnt(6)\n\t"
"v_mfma_f32_16x16x32_bf16 v[52:55], v[20:23], v[108:111], v[52:55]\n\t"
"ds_read_b128 v[16:19], v236 offset:25600\n\t"
"v_mfma_f32_16x16x32_bf16 v[36:39], v[20:23], v[104:107], v[36:39]\n\t"
"s_waitcnt lgkmcnt(6)\n\t"
"v_mfma_f32_16x16x32_bf16 v[40:43], v[24:27], v[104:107], v[40:43]\n\t"
"ds_read_b128 v[20:23], v236 offset:27648\n\t"
"v_mfma_f32_16x16x32_bf16 v[56:59], v[24:27], v[108:111], v[56:59]\n\t"
"s_waitcnt lgkmcnt(6)\n\t"
"v_mfma_f32_16x16x32_bf16 v[60:63], v[28:31], v[108:111], v[60:63]\n\t"
"ds_read_b128 v[24:27], v236 offset:29696\n\t"
"v_mfma_f32_16x16x32_bf16 v[44:47], v[28:31], v[104:107], v[44:47]\n\t"
"ds_read_b128 v[28:31], v236 offset:31744\n\t"
"s_waitcnt lgkmcnt(7)\n\t"
"v_mfma_f32_16x16x32_bf16 v[32:35], v[0:3], v[112:115], v[32:35]\n\t"
"v_mfma_f32_16x16x32_bf16 v[48:51], v[0:3], v[116:119], v[48:51]\n\t"
"s_waitcnt lgkmcnt(6)\n\t"
"v_mfma_f32_16x16x32_bf16 v[52:55], v[4:7], v[116:119], v[52:55]\n\t"
"ds_read_b128 v[0:3], v236 offset:32768\n\t"
"v_mfma_f32_16x16x32_bf16 v[36:39], v[4:7], v[112:115], v[36:39]\n\t"
"s_waitcnt lgkmcnt(6)\n\t"
"v_mfma_f32_16x16x32_bf16 v[40:43], v[8:11], v[112:115], v[40:43]\n\t"
"ds_read_b128 v[4:7], v236 offset:34816\n\t"
"v_mfma_f32_16x16x32_bf16 v[56:59], v[8:11], v[116:119], v[56:59]\n\t"
"s_waitcnt lgkmcnt(6)\n\t"
"v_mfma_f32_16x16x32_bf16 v[60:63], v[12:15], v[116:119], v[60:63]\n\t"
"ds_read_b128 v[8:11], v236 offset:36864\n\t"
"v_mfma_f32_16x16x32_bf16 v[44:47], v[12:15], v[112:115], v[44:47]\n\t"
"s_waitcnt lgkmcnt(6)\n\t"
"v_mfma_f32_16x16x32_bf16 v[32:35], v[16:19], v[120:123], v[32:35]\n\t"
"ds_read_b128 v[12:15], v236 offset:38912\n\t"
"v_mfma_f32_16x16x32_bf16 v[48:51], v[16:19], v[124:127], v[48:51]\n\t"
"s_waitcnt lgkmcnt(6)\n\t"
"v_mfma_f32_16x16x32_bf16 v[52:55], v[20:23], v[124:127], v[52:55]\n\t"
"ds_read_b128 v[16:19], v236 offset:33792\n\t"
"v_mfma_f32_16x16x32_bf16 v[36:39], v[20:23], v[120:123], v[36:39]\n\t"
"s_waitcnt lgkmcnt(6)\n\t"
"v_mfma_f32_16x16x32_bf16 v[40:43], v[24:27], v[120:123], v[40:43]\n\t"
"ds_read_b128 v[20:23], v236 offset:35840\n\t"
"v_mfma_f32_16x16x32_bf16 v[56:59], v[24:27], v[124:127], v[56:59]\n\t"
"s_waitcnt lgkmcnt(6)\n\t"
"v_mfma_f32_16x16x32_bf16 v[60:63], v[28:31], v[124:127], v[60:63]\n\t"
"ds_read_b128 v[24:27], v236 offset:37888\n\t"
"v_mfma_f32_16x16x32_bf16 v[44:47], v[28:31], v[120:123], v[44:47]\n\t"
"ds_read_b128 v[28:31], v236 offset:39936\n\t"
"s_waitcnt lgkmcnt(7)\n\t"
"v_mfma_f32_16x16x32_bf16 v[32:35], v[0:3], v[128:131], v[32:35]\n\t"
"v_mfma_f32_16x16x32_bf16 v[48:51], v[0:3], v[132:135], v[48:51]\n\t"
"s_waitcnt lgkmcnt(6)\n\t"
"v_mfma_f32_16x16x32_bf16 v[52:55], v[4:7], v[132:135], v[52:55]\n\t"
"ds_read_b128 v[0:3], v236 offset:40960\n\t"
"v_mfma_f32_16x16x32_bf16 v[36:39], v[4:7], v[128:131], v[36:39]\n\t"
"s_waitcnt lgkmcnt(6)\n\t"
"v_mfma_f32_16x16x32_bf16 v[40:43], v[8:11], v[128:131], v[40:43]\n\t"
"ds_read_b128 v[4:7], v236 offset:43008\n\t"
"v_mfma_f32_16x16x32_bf16 v[56:59], v[8:11], v[132:135], v[56:59]\n\t"
"s_waitcnt lgkmcnt(6)\n\t"
"v_mfma_f32_16x16x32_bf16 v[60:63], v[12:15], v[132:135], v[60:63]\n\t"
"ds_read_b128 v[8:11], v236 offset:45056\n\t"
"v_mfma_f32_16x16x32_bf16 v[44:47], v[12:15], v[128:131], v[44:47]\n\t"
"s_waitcnt lgkmcnt(6)\n\t"
"v_mfma_f32_16x16x32_bf16 v[32:35], v[16:19], v[136:139], v[32:35]\n\t"
"ds_read_b128 v[12:15], v236 offset:47104\n\t"
"v_mfma_f32_16x16x32_bf16 v[48:51], v[16:19], v[140:143], v[48:51]\n\t"
"s_waitcnt lgkmcnt(6)\n\t"
"v_mfma_f32_16x16x32_bf16 v[52:55], v[20:23], v[140:143], v[52:55]\n\t"
"ds_read_b128 v[16:19], v236 offset:41984\n\t"
"v_mfma_f32_16x16x32_bf16 v[36:39], v[20:23], v[136:139], v[36:39]\n\t"
"s_waitcnt lgkmcnt(6)\n\t"
"v_mfma_f32_16x16x32_bf16 v[40:43], v[24:27], v[136:139], v[40:43]\n\t"
"ds_read_b128 v[20:23], v236 offset:44032\n\t"
"v_mfma_f32_16x16x32_bf16 v[56:59], v[24:27], v[140:143], v[56:59]\n\t"
"s_waitcnt lgkmcnt(6)\n\t"
"v_mfma_f32_16x16x32_bf16 v[60:63], v[28:31], v[140:143], v[60:63]\n\t"
"ds_read_b128 v[24:27], v236 offset:46080\n\t"
"v_mfma_f32_16x16x32_bf16 v[44:47], v[28:31], v[136:139], v[44:47]\n\t"
"ds_read_b128 v[28:31], v236 offset:48128\n\t"
"v_add_u32 v236, 0xffff0000, v236\n\t"
"s_waitcnt lgkmcnt(7)\n\t"
"v_mfma_f32_16x16x32_bf16 v[32:35], v[0:3], v[144:147], v[32:35]\n\t"
"v_mfma_f32_16x16x32_bf16 v[48:51], v[0:3], v[148:151], v[48:51]\n\t"
"s_waitcnt lgkmcnt(6)\n\t"
"v_mfma_f32_16x16x32_bf16 v[52:55], v[4:7], v[148:151], v[52:55]\n\t"
"ds_read_b128 v[0:3], v236 offset:0\n\t"
"v_mfma_f32_16x16x32_bf16 v[36:39], v[4:7], v[144:147], v[36:39]\n\t"
"s_waitcnt lgkmcnt(6)\n\t"
"v_mfma_f32_16x16x32_bf16 v[40:43], v[8:11], v[144:147], v[40:43]\n\t"
"ds_read_b128 v[4:7], v236 offset:2048\n\t"
"v_mfma_f32_16x16x32_bf16 v[56:59], v[8:11], v[148:151], v[56:59]\n\t"
"s_waitcnt lgkmcnt(6)\n\t"
"v_mfma_f32_16x16x32_bf16 v[60:63], v[12:15], v[148:151], v[60:63]\n\t"
"ds_read_b128 v[8:11], v236 offset:4096\n\t"
"v_mfma_f32_16x16x32_bf16 v[44:47], v[12:15], v[144:147], v[44:47]\n\t"
"s_waitcnt lgkmcnt(6)\n\t"
"v_mfma_f32_16x16x32_bf16 v[32:35], v[16:19], v[152:155], v[32:35]\n\t"
"ds_read_b128 v[12:15], v236 offset:6144\n\t"
"v_mfma_f32_16x16x32_bf16 v[48:51], v[16:19], v[156:159], v[48:51]\n\t"
"s_waitcnt lgkmcnt(6)\n\t"
"v_mfma_f32_16x16x32_bf16 v[52:55], v[20:23], v[156:159], v[52:55]\n\t"
"ds_read_b128 v[16:19], v236 offset:1024\n\t"
"v_mfma_f32_16x16x32_bf16 v[36:39], v[20:23], v[152:155], v[36:39]\n\t"
"s_waitcnt lgkmcnt(6)\n\t"
"v_mfma_f32_16x16x32_bf16 v[40:43], v[24:27], v[152:155], v[40:43]\n\t"
"ds_read_b128 v[20:23], v236 offset:3072\n\t"
"v_mfma_f32_16x16x32_bf16 v[56:59], v[24:27], v[156:159], v[56:59]\n\t"
"s_waitcnt lgkmcnt(6)\n\t"
"v_mfma_f32_16x16x32_bf16 v[60:63], v[28:31], v[156:159], v[60:63]\n\t"
"ds_read_b128 v[24:27], v236 offset:5120\n\t"
"v_mfma_f32_16x16x32_bf16 v[44:47], v[28:31], v[152:155], v[44:47]\n\t"
"ds_read_b128 v[28:31], v236 offset:7168\n\t"
"s_waitcnt lgkmcnt(7)\n\t"
"v_mfma_f32_16x16x32_bf16 v[32:35], v[0:3], v[160:163], v[32:35]\n\t"
"v_mfma_f32_16x16x32_bf16 v[48:51], v[0:3], v[164:167], v[48:51]\n\t"
"s_waitcnt lgkmcnt(6)\n\t"
"v_mfma_f32_16x16x32_bf16 v[52:55], v[4:7], v[164:167], v[52:55]\n\t"
"ds_read_b128 v[0:3], v236 offset:8192\n\t"
"v_mfma_f32_16x16x32_bf16 v[36:39], v[4:7], v[160:163], v[36:39]\n\t"
"s_waitcnt lgkmcnt(6)\n\t"
"v_mfma_f32_16x16x32_bf16 v[40:43], v[8:11], v[160:163], v[40:43]\n\t"
"ds_read_b128 v[4:7], v236 offset:10240\n\t"
"v_mfma_f32_16x16x32_bf16 v[56:59], v[8:11], v[164:167], v[56:59]\n\t"
"s_waitcnt lgkmcnt(6)\n\t"
"v_mfma_f32_16x16x32_bf16 v[60:63], v[12:15], v[164:167], v[60:63]\n\t"
"ds_read_b128 v[8:11], v236 offset:12288\n\t"
"v_mfma_f32_16x16x32_bf16 v[44:47], v[12:15], v[160:163], v[44:47]\n\t"
"s_waitcnt lgkmcnt(6)\n\t"
"v_mfma_f32_16x16x32_bf16 v[32:35], v[16:19], v[168:171], v[32:35]\n\t"
"ds_read_b128 v[12:15], v236 offset:14336\n\t"
"v_mfma_f32_16x16x32_bf16 v[48:51], v[16:19], v[172:175], v[48:51]\n\t"
"s_waitcnt lgkmcnt(6)\n\t"
"v_mfma_f32_16x16x32_bf16 v[52:55], v[20:23], v[172:175], v[52:55]\n\t"
"ds_read_b128 v[16:19], v236 offset:9216\n\t"
"v_mfma_f32_16x16x32_bf16 v[36:39], v[20:23], v[168:171], v[36:39]\n\t"
"s_waitcnt lgkmcnt(6)\n\t"
"v_mfma_f32_16x16x32_bf16 v[40:43], v[24:27], v[168:171], v[40:43]\n\t"
"ds_read_b128 v[20:23], v236 offset:11264\n\t"
"v_mfma_f32_16x16x32_bf16 v[56:59], v[24:27], v[172:175], v[56:59]\n\t"
"s_waitcnt lgkmcnt(6)\n\t"
"v_mfma_f32_16x16x32_bf16 v[60:63], v[28:31], v[172:175], v[60:63]\n\t"
"ds_read_b128 v[24:27], v236 offset:13312\n\t"
"v_mfma_f32_16x16x32_bf16 v[44:47], v[28:31], v[168:171], v[44:47]\n\t"
"ds_read_b128 v[28:31], v236 offset:15360\n\t"
"s_waitcnt lgkmcnt(7)\n\t"
"v_mfma_f32_16x16x32_bf16 v[32:35], v[0:3], v[176:179], v[32:35]\n\t"
"ds_read_b128 v[64:67], v248 offset:0\n\t"
"v_mfma_f32_16x16x32_bf16 v[48:51], v[0:3], v[180:183], v[48:51]\n\t"
"ds_read_b128 v[68:71], v248 offset:2048\n\t"
"s_waitcnt lgkmcnt(8)\n\t"
"v_mfma_f32_16x16x32_bf16 v[52:55], v[4:7], v[180:183], v[52:55]\n\t"
"ds_read_b128 v[0:3], v236 offset:16384\n\t"
"v_mfma_f32_16x16x32_bf16 v[36:39], v[4:7], v[176:179], v[36:39]\n\t"
"ds_read_b128 v[72:75], v249 offset:0\n\t"
"s_waitcnt lgkmcnt(9)\n\t"
"v_mfma_f32_16x16x32_bf16 v[40:43], v[8:11], v[176:179], v[40:43]\n\t"
"ds_read_b128 v[4:7], v236 offset:18432\n\t"
"v_mfma_f32_16x16x32_bf16 v[56:59], v[8:11], v[180:183], v[56:59]\n\t"
"ds_read_b128 v[76:79], v249 offset:2048\n\t"
"s_waitcnt lgkmcnt(10)\n\t"
"v_mfma_f32_16x16x32_bf16 v[60:63], v[12:15], v[180:183], v[60:63]\n\t"
"ds_read_b128 v[8:11], v236 offset:20480\n\t"
"v_mfma_f32_16x16x32_bf16 v[44:47], v[12:15], v[176:179], v[44:47]\n\t"
"s_waitcnt lgkmcnt(10)\n\t"
"v_mfma_f32_16x16x32_bf16 v[32:35], v[16:19], v[184:187], v[32:35]\n\t"
"ds_read_b128 v[12:15], v236 offset:22528\n\t"
"v_mfma_f32_16x16x32_bf16 v[48:51], v[16:19], v[188:191], v[48:51]\n\t"
"s_waitcnt lgkmcnt(10)\n\t"
"v_mfma_f32_16x16x32_bf16 v[52:55], v[20:23], v[188:191], v[52:55]\n\t"
"ds_read_b128 v[16:19], v236 offset:17408\n\t"
"v_mfma_f32_16x16x32_bf16 v[36:39], v[20:23], v[184:187], v[36:39]\n\t"
"s_waitcnt lgkmcnt(10)\n\t"
"v_mfma_f32_16x16x32_bf16 v[40:43], v[24:27], v[184:187], v[40:43]\n\t"
"ds_read_b128 v[20:23], v236 offset:19456\n\t"
"v_mfma_f32_16x16x32_bf16 v[56:59], v[24:27], v[188:191], v[56:59]\n\t"
"s_waitcnt lgkmcnt(10)\n\t"
"v_mfma_f32_16x16x32_bf16 v[60:63], v[28:31], v[188:191], v[60:63]\n\t"
"ds_read_b128 v[24:27], v236 offset:21504\n\t"
"v_mfma_f32_16x16x32_bf16 v[44:47], v[28:31], v[184:187], v[44:47]\n\t"
"ds_read_b128 v[28:31], v236 offset:23552\n\t"
"s_waitcnt lgkmcnt(9)\n\t"
"v_mfma_f32_16x16x32_bf16 v[32:35], v[0:3], v[192:195], v[32:35]\n\t"
"v_mfma_f32_16x16x32_bf16 v[48:51], v[0:3], v[196:199], v[48:51]\n\t"
"s_waitcnt lgkmcnt(7)\n\t"
"v_mfma_f32_16x16x32_bf16 v[52:55], v[4:7], v[196:199], v[52:55]\n\t"
"ds_read_b128 v[0:3], v236 offset:24576\n\t"
"v_mfma_f32_16x16x32_bf16 v[36:39], v[4:7], v[192:195], v[36:39]\n\t"
"s_waitcnt lgkmcnt(6)\n\t"
"v_mfma_f32_16x16x32_bf16 v[40:43], v[8:11], v[192:195], v[40:43]\n\t"
"ds_read_b128 v[4:7], v236 offset:26624\n\t"
"v_mfma_f32_16x16x32_bf16 v[56:59], v[8:11], v[196:199], v[56:59]\n\t"
"s_waitcnt lgkmcnt(6)\n\t"
"v_mfma_f32_16x16x32_bf16 v[60:63], v[12:15], v[196:199], v[60:63]\n\t"
"ds_read_b128 v[8:11], v236 offset:28672\n\t"
"v_mfma_f32_16x16x32_bf16 v[44:47], v[12:15], v[192:195], v[44:47]\n\t"
"s_waitcnt lgkmcnt(6)\n\t"
"v_mfma_f32_16x16x32_bf16 v[32:35], v[16:19], v[200:203], v[32:35]\n\t"
"ds_read_b128 v[12:15], v236 offset:30720\n\t"
"v_mfma_f32_16x16x32_bf16 v[48:51], v[16:19], v[204:207], v[48:51]\n\t"
"s_waitcnt lgkmcnt(6)\n\t"
"v_mfma_f32_16x16x32_bf16 v[52:55], v[20:23], v[204:207], v[52:55]\n\t"
"ds_read_b128 v[16:19], v236 offset:25600\n\t"
"v_mfma_f32_16x16x32_bf16 v[36:39], v[20:23], v[200:203], v[36:39]\n\t"
"s_waitcnt lgkmcnt(6)\n\t"
"v_mfma_f32_16x16x32_bf16 v[40:43], v[24:27], v[200:203], v[40:43]\n\t"
"ds_read_b128 v[20:23], v236 offset:27648\n\t"
"v_mfma_f32_16x16x32_bf16 v[56:59], v[24:27], v[204:207], v[56:59]\n\t"
"s_waitcnt lgkmcnt(6)\n\t"
"v_mfma_f32_16x16x32_bf16 v[60:63], v[28:31], v[204:207], v[60:63]\n\t"
"ds_read_b128 v[24:27], v236 offset:29696\n\t"
"v_mfma_f32_16x16x32_bf16 v[44:47], v[28:31], v[200:203], v[44:47]\n\t"
"ds_read_b128 v[28:31], v236 offset:31744\n\t"
"v_add_u32 v236, 0x20000, v236\n\t"
"s_waitcnt lgkmcnt(7)\n\t"
"v_mfma_f32_16x16x32_bf16 v[32:35], v[0:3], v[208:211], v[32:35]\n\t"
"v_mfma_f32_16x16x32_bf16 v[48:51], v[0:3], v[212:215], v[48:51]\n\t"
"s_waitcnt lgkmcnt(6)\n\t"
"v_mfma_f32_16x16x32_bf16 v[52:55], v[4:7], v[212:215], v[52:55]\n\t"
"ds_read_b128 v[0:3], v236 offset:0\n\t"
"v_mfma_f32_16x16x32_bf16 v[36:39], v[4:7], v[208:211], v[36:39]\n\t"
"s_waitcnt lgkmcnt(6)\n\t"
"v_mfma_f32_16x16x32_bf16 v[40:43], v[8:11], v[208:211], v[40:43]\n\t"
"ds_read_b128 v[4:7], v236 offset:2048\n\t"
"v_mfma_f32_16x16x32_bf16 v[56:59], v[8:11], v[212:215], v[56:59]\n\t"
"s_waitcnt lgkmcnt(6)\n\t"
"v_mfma_f32_16x16x32_bf16 v[60:63], v[12:15], v[212:215], v[60:63]\n\t"
"ds_read_b128 v[8:11], v236 offset:4096\n\t"
"v_mfma_f32_16x16x32_bf16 v[44:47], v[12:15], v[208:211], v[44:47]\n\t"
"s_waitcnt lgkmcnt(6)\n\t"
"v_mfma_f32_16x16x32_bf16 v[32:35], v[16:19], v[216:219], v[32:35]\n\t"
"ds_read_b128 v[12:15], v236 offset:6144\n\t"
"v_mfma_f32_16x16x32_bf16 v[48:51], v[16:19], v[220:223], v[48:51]\n\t"
"s_waitcnt lgkmcnt(6)\n\t"
"v_mfma_f32_16x16x32_bf16 v[52:55], v[20:23], v[220:223], v[52:55]\n\t"
"ds_read_b128 v[16:19], v236 offset:1024\n\t"
"v_mfma_f32_16x16x32_bf16 v[36:39], v[20:23], v[216:219], v[36:39]\n\t"
"s_waitcnt lgkmcnt(6)\n\t"
"v_mfma_f32_16x16x32_bf16 v[40:43], v[24:27], v[216:219], v[40:43]\n\t"
"ds_read_b128 v[20:23], v236 offset:3072\n\t"
"v_mfma_f32_16x16x32_bf16 v[56:59], v[24:27], v[220:223], v[56:59]\n\t"
"s_waitcnt lgkmcnt(6)\n\t"
"v_mfma_f32_16x16x32_bf16 v[60:63], v[28:31], v[220:223], v[60:63]\n\t"
"ds_read_b128 v[24:27], v236 offset:5120\n\t"
"v_mfma_f32_16x16x32_bf16 v[44:47], v[28:31], v[216:219], v[44:47]\n\t"
"ds_read_b128 v[28:31], v236 offset:7168\n\t"
"s_waitcnt lgkmcnt(7)\n\t"
"v_mfma_f32_16x16x32_bf16 v[32:35], v[0:3], v[224:227], v[32:35]\n\t"
"v_mfma_f32_16x16x32_bf16 v[48:51], v[0:3], v[228:231], v[48:51]\n\t"
"s_waitcnt lgkmcnt(6)\n\t"
"v_mfma_f32_16x16x32_bf16 v[52:55], v[4:7], v[228:231], v[52:55]\n\t"
"ds_read_b128 v[0:3], v236 offset:8192\n\t"
"v_mfma_f32_16x16x32_bf16 v[36:39], v[4:7], v[224:227], v[36:39]\n\t"
"s_waitcnt lgkmcnt(6)\n\t"
"v_mfma_f32_16x16x32_bf16 v[40:43], v[8:11], v[224:227], v[40:43]\n\t"
"ds_read_b128 v[4:7], v236 offset:10240\n\t"
"v_mfma_f32_16x16x32_bf16 v[56:59], v[8:11], v[228:231], v[56:59]\n\t"
"s_waitcnt lgkmcnt(6)\n\t"
"v_mfma_f32_16x16x32_bf16 v[60:63], v[12:15], v[228:231], v[60:63]\n\t"
"ds_read_b128 v[8:11], v236 offset:12288\n\t"
"v_mfma_f32_16x16x32_bf16 v[44:47], v[12:15], v[224:227], v[44:47]\n\t"
"s_waitcnt lgkmcnt(6)\n\t"
"v_mfma_f32_16x16x32_bf16 v[32:35], v[16:19], v[232:235], v[32:35]\n\t"
"ds_read_b128 v[12:15], v236 offset:14336\n\t"
"v_mfma_f32_16x16x32_bf16 v[48:51], v[16:19], v[240:243], v[48:51]\n\t"
"s_waitcnt lgkmcnt(6)\n\t"
"v_mfma_f32_16x16x32_bf16 v[52:55], v[20:23], v[240:243], v[52:55]\n\t"
"ds_read_b128 v[16:19], v236 offset:9216\n\t"
"v_mfma_f32_16x16x32_bf16 v[36:39], v[20:23], v[232:235], v[36:39]\n\t"
"s_waitcnt lgkmcnt(6)\n\t"
"v_mfma_f32_16x16x32_bf16 v[40:43], v[24:27], v[232:235], v[40:43]\n\t"
"ds_read_b128 v[20:23], v236 offset:11264\n\t"
"v_mfma_f32_16x16x32_bf16 v[56:59], v[24:27], v[240:243], v[56:59]\n\t"
"s_waitcnt lgkmcnt(6)\n\t"
"v_mfma_f32_16x16x32_bf16 v[60:63], v[28:31], v[240:243], v[60:63]\n\t"
"ds_read_b128 v[24:27], v236 offset:13312\n\t"
"v_mfma_f32_16x16x32_bf16 v[44:47], v[28:31], v[232:235], v[44:47]\n\t"
"ds_read_b128 v[28:31], v236 offset:15360\n\t"
"s_waitcnt lgkmcnt(7)\n\t"
"v_mfma_f32_16x16x32_bf16 v[32:35], v[0:3], v[64:67], v[32:35]\n\t"
"v_mfma_f32_16x16x32_bf16 v[48:51], v[0:3], v[68:71], v[48:51]\n\t"
"s_waitcnt lgkmcnt(6)\n\t"
"v_mfma_f32_16x16x32_bf16 v[52:55], v[4:7], v[68:71], v[52:55]\n\t"
"v_mfma_f32_16x16x32_bf16 v[36:39], v[4:7], v[64:67], v[36:39]\n\t"
"s_waitcnt lgkmcnt(3)\n\t"
"v_mfma_f32_16x16x32_bf16 v[32:35], v[16:19], v[72:75], v[32:35]\n\t"
"v_mfma_f32_16x16x32_bf16 v[48:51], v[16:19], v[76:79], v[48:51]\n\t"
"s_waitcnt lgkmcnt(2)\n\t"
"v_mfma_f32_16x16x32_bf16 v[52:55], v[20:23], v[76:79], v[52:55]\n\t"
"v_mfma_f32_16x16x32_bf16 v[36:39], v[20:23], v[72:75], v[36:39]\n\t"
"v_mfma_f32_16x16x32_bf16 v[40:43], v[8:11], v[64:67], v[40:43]\n\t"
"v_mfma_f32_16x16x32_bf16 v[56:59], v[8:11], v[68:71], v[56:59]\n\t"
"v_mfma_f32_16x16x32_bf16 v[60:63], v[12:15], v[68:71], v[60:63]\n\t"
"v_mfma_f32_16x16x32_bf16 v[44:47], v[12:15], v[64:67], v[44:47]\n\t"
"s_waitcnt lgkmcnt(1)\n\t"
"v_mfma_f32_16x16x32_bf16 v[40:43], v[24:27], v[72:75], v[40:43]\n\t"
"v_mfma_f32_16x16x32_bf16 v[56:59], v[24:27], v[76:79], v[56:59]\n\t"
"s_waitcnt lgkmcnt(0)\n\t"
"v_mfma_f32_16x16x32_bf16 v[60:63], v[28:31], v[76:79], v[60:63]\n\t"
"v_mfma_f32_16x16x32_bf16 v[44:47], v[28:31], v[72:75], v[44:47]\n\t"
"ds_write_b128 v237, v[32:35] offset:0\n\t"
"ds_write_b128 v237, v[48:51] offset:4096\n\t"
"ds_write_b128 v237, v[52:55] offset:5120\n\t"
"ds_write_b128 v237, v[36:39] offset:1024\n\t"
"s_nop 1\n\t"
"ds_write_b128 v237, v[40:43] offset:2048\n\t"
"ds_write_b128 v237, v[56:59] offset:6144\n\t"
"ds_write_b128 v237, v[60:63] offset:7168\n\t"
"ds_write_b128 v237, v[44:47] offset:3072\n\t"
"s_waitcnt lgkmcnt(0)\n\t"
"s_barrier\n\t"
"s_or_b32 s59, s55, s45\n\t"
"s_cmp_lg_u32 s59, 0\n\t"
"s_cbranch_scc1 L_noep_%=\n\t"
"s_add_u32 s59, s54, 1\n\t"
"v_mov_b32 v20, s59\n\t"
"v_mov_b32 v21, 33583104\n\t"
"s_mov_b64 exec, 1\n\t"
"buffer_store_dword v20, v21, s[24:27], 0 offen sc1\n\t"
"s_mov_b64 exec, -1\n\t"
"L_noep_%=:\n\t"
"s_waitcnt vmcnt(0)\n\t"
"s_endpgm\n\t"
"L_compute_%=:\n\t"
"s_cmp_eq_u32 s45, 0\n\t"
"s_cselect_b32 s60, 7, 3\n\t"
"v_lshrrev_b32 v16, 2, v252\n\t"
"v_and_b32 v16, s60, v16\n\t"
"v_and_b32 v17, 3, v252\n\t"
"v_lshlrev_b32 v17, 7, v17\n\t"
"v_lshl_add_u32 v28, v16, 11, v17\n\t"
"s_lshl_b32 s59, s5, 5\n\t"
"s_add_u32 s59, s59, s4\n\t"
"s_lshl_b32 s59, s59, 13\n\t"
"s_lshl_b32 s60, s55, 14\n\t"
"s_cmp_eq_u32 s45, 0\n\t"
"s_cselect_b32 s59, s60, s59\n\t"
"s_cmp_eq_u32 s45, 2\n\t"
"s_cselect_b32 s60, 1024, 0\n\t"
"s_add_u32 s59, s59, s60\n\t"
"v_add_u32 v28, s59, v28\n\t"
"s_mov_b32 s67, 512\n\t"
"s_cmp_lg_u32 s45, 3\n\t"
"s_cbranch_scc1 L_cla18_%=\n\t"
"v_bfe_u32 v16, v252, 1, 2\n\t"
"v_bfe_u32 v17, v252, 3, 1\n\t"
"v_lshl_add_u32 v16, v17, 7, v16\n\t"
"s_lshl_b32 s59, s4, 2\n\t"
"v_add_u32 v16, s59, v16\n\t"
"v_mul_u32_u24 v16, 6144, v16\n\t"
"v_min_u32 v17, 2, v254\n\t"
"v_lshl_add_u32 v16, v17, 11, v16\n\t"
"v_and_b32 v17, 1, v252\n\t"
"v_lshl_add_u32 v16, v17, 7, v16\n\t"
"s_lshl_b32 s59, s5, 8\n\t"
"v_add_u32 v28, s59, v16\n\t"
"s_mov_b32 s67, 1572864\n\t"
"L_cla18_%=:\n\t"
"s_waitcnt lgkmcnt(0)\n\t"
"s_sub_u32 s20, s20, 1024\n\t"
"s_subb_u32 s21, s21, 0\n\t"
"s_and_b32 s9, s9, 0xffff\n\t"
"s_and_b32 s17, s17, 0xffff\n\t"
"s_and_b32 s21, s21, 0xffff\n\t"
"s_and_b32 s13, s13, 0xffff\n\t"
"s_mov_b64 s[72:73], s[8:9]\n\t"
"s_mov_b32 s74, s10\n\t"
"s_mov_b32 s75, 0x20000\n\t"
"s_cmp_eq_u32 s45, 1\n\t"
"s_cselect_b64 s[72:73], s[16:17], s[72:73]\n\t"
"s_cselect_b32 s74, s18, s74\n\t"
"s_cmp_eq_u32 s45, 2\n\t"
"s_cselect_b64 s[72:73], s[20:21], s[72:73]\n\t"
"s_cselect_b32 s74, s22, s74\n\t"
"s_cmp_eq_u32 s45, 3\n\t"
"s_cselect_b64 s[72:73], s[12:13], s[72:73]\n\t"
"s_cselect_b32 s74, s14, s74\n\t"
"s_mul_i32 s59, s67, 0\n\t"
"buffer_load_dword v245, v28, s[72:75], s59 offen\n\t"
"s_lshr_b32 s48, s5, 1\n\t"
"s_and_b32 s49, s5, 1\n\t"
"s_lshl_b32 s49, s49, 2\n\t"
"s_and_b32 s61, s4, 3\n\t"
"s_add_u32 s49, s49, s61\n\t"
"s_lshr_b32 s50, s4, 2\n\t"
"s_and_b32 s51, s48, 1\n\t"
"s_lshr_b32 s52, s48, 1\n\t"
"s_cmp_lt_u32 s48, 2\n\t"
"s_cselect_b32 s28, s80, s82\n\t"
"s_cselect_b32 s29, s81, s83\n\t"
"s_mov_b32 s28, s28\n\t"
"s_and_b32 s29, s29, 0xffff\n\t"
"s_mov_b32 s30, 0x800000\n\t"
"s_mov_b32 s31, 0x20000\n\t"
"s_mov_b32 s32, s84\n\t"
"s_and_b32 s33, s85, 0xffff\n\t"
"s_mov_b32 s34, 0x200000\n\t"
"s_mov_b32 s35, 0x20000\n\t"
"s_mov_b32 s40, s86\n\t"
"s_and_b32 s41, s87, 0xffff\n\t"
"s_mov_b32 s42, 2048\n\t"
"s_mov_b32 s43, 0x20000\n\t"
"s_mov_b32 s36, s88\n\t"
"s_and_b32 s37, s89, 0xffff\n\t"
"s_mov_b32 s38, 0x400000\n\t"
"s_mov_b32 s39, 0x20000\n\t"
"s_mov_b32 s24, s90\n\t"
"s_and_b32 s25, s91, 0xffff\n\t"
"s_mov_b32 s26, 0x4000000\n\t"
"s_mov_b32 s27, 0x20000\n\t"
"s_add_u32 s90, s90, 33583104\n\t"
"s_addc_u32 s91, s91, 0\n\t"
"s_load_dword s54, s[90:91], 0x0\n\t"
"v_lshrrev_b32 v240, 1, v253\n\t"
"v_xor_b32 v240, v240, v254\n\t"
"v_lshlrev_b32 v240, 4, v240\n\t"
"v_lshl_add_u32 v232, v253, 7, v240\n\t"
"s_lshl_b32 s59, s45, 13\n\t"
"v_add_u32 v232, s59, v232\n\t"
"v_xor_b32 v233, 64, v232\n\t"
"v_lshrrev_b32 v241, 3, v253\n\t"
"v_lshlrev_b32 v241, 11, v241\n\t"
"v_and_b32 v242, 7, v253\n\t"
"v_lshl_add_u32 v241, v242, 7, v241\n\t"
"v_add_u32 v241, v241, v240\n\t"
"v_add_u32 v236, 45056, v241\n\t"
"v_xor_b32 v237, 64, v236\n\t"
"v_lshrrev_b32 v241, 1, v254\n\t"
"v_mul_u32_u24 v241, 3072, v241\n\t"
"v_and_b32 v242, 1, v254\n\t"
"v_lshrrev_b32 v243, 2, v253\n\t"
"v_lshl_add_u32 v243, v242, 3, v243\n\t"
"v_lshl_add_u32 v241, v243, 6, v241\n\t"
"v_bfe_u32 v243, v253, 1, 1\n\t"
"v_lshlrev_b32 v242, 1, v242\n\t"
"v_xor_b32 v243, v243, v242\n\t"
"v_lshl_add_u32 v241, v243, 4, v241\n\t"
"v_and_b32 v243, 1, v253\n\t"
"v_lshl_add_u32 v241, v243, 3, v241\n\t"
"v_add_u32 v234, 32768, v241\n\t"
"v_xor_b32 v235, 32, v234\n\t"
"v_lshrrev_b32 v0, 1, v254\n\t"
"v_lshlrev_b32 v0, 10, v0\n\t"
"v_and_b32 v1, 1, v254\n\t"
"v_lshl_add_u32 v0, v1, 9, v0\n\t"
"v_lshrrev_b32 v1, 3, v253\n\t"
"v_lshlrev_b32 v1, 4, v1\n\t"
"v_and_b32 v2, 7, v253\n\t"
"v_add_u32 v1, v1, v2\n\t"
"v_lshl_add_u32 v240, v1, 4, v0\n\t"
"v_lshl_add_u32 v241, v253, 4, v0\n\t"
"v_add_u32 v241, 2048, v241\n\t"
"v_lshlrev_b32 v238, 4, v252\n\t"
"s_lshl_b32 s59, s45, 13\n\t"
"v_add_u32 v240, s59, v240\n\t"
"v_add_u32 v241, s59, v241\n\t"
"v_lshrrev_b32 v1, 2, v253\n\t"
"v_lshlrev_b32 v1, 3, v1\n\t"
"v_and_b32 v2, 3, v253\n\t"
"v_add_u32 v1, v1, v2\n\t"
"v_lshl_add_u32 v242, v1, 4, v0\n\t"
"s_lshl_b32 s59, s45, 12\n\t"
"s_add_u32 s59, s59, 131072\n\t"
"v_add_u32 v242, s59, v242\n\t"
"v_add_u32 v243, s59, v238\n\t"
"s_lshl_b32 s59, s45, 13\n\t"
"s_add_u32 s59, s59, 98304\n\t"
"v_add_u32 v244, s59, v238\n\t"
"v_lshrrev_b32 v0, 1, v253\n\t"
"v_xor_b32 v0, v0, v254\n\t"
"v_lshlrev_b32 v0, 4, v0\n\t"
"v_lshl_add_u32 v248, v253, 7, v0\n\t"
"s_and_b32 s59, s45, 1\n\t"
"s_lshl_b32 s59, s59, 13\n\t"
"s_add_u32 s59, s59, 0\n\t"
"v_add_u32 v248, s59, v248\n\t"
"v_xor_b32 v249, 64, v248\n\t"
"s_add_u32 s91, s59, 0\n\t"
"s_lshr_b32 s59, s45, 1\n\t"
"s_lshl_b32 s59, s59, 12\n\t"
"s_add_u32 s59, s59, 98304\n\t"
"v_add_u32 v250, s59, v238\n\t"
"s_mov_b32 s92, 32768\n\t"
"s_mov_b32 s95, 16384\n\t"
"s_mov_b32 s93, 32768\n\t"
"s_mov_b32 s96, 4294918144\n\t"
"s_mov_b32 s94, 4294901760\n\t"
"s_mov_b32 s97, 32768\n\t"
"s_lshl_b32 s59, s46, 1\n\t"
"s_lshr_b32 s60, s45, 1\n\t"
"s_add_u32 s59, s59, s60\n\t"
"s_lshl_b32 s59, s59, 4\n\t"
"s_lshr_b32 s60, s47, 1\n\t"
"s_add_u32 s59, s59, s60\n\t"
"s_lshl_b32 s59, s59, 14\n\t"
"s_and_b32 s60, s45, 1\n\t"
"s_lshl_b32 s60, s60, 13\n\t"
"s_add_u32 s59, s59, s60\n\t"
"s_and_b32 s60, s47, 1\n\t"
"s_lshl_b32 s60, s60, 10\n\t"
"s_add_u32 s59, s59, s60\n\t"
"s_add_u32 s98, s59, 16777216\n\t"
"s_add_u32 s99, s98, 4096\n\t"
"v_and_b32 v0, 1, v254\n\t"
"v_lshlrev_b32 v0, 11, v0\n\t"
"v_lshrrev_b32 v1, 1, v254\n\t"
"v_lshl_add_u32 v0, v1, 8, v0\n\t"
"v_lshl_add_u32 v251, v253, 4, v0\n\t"
"s_lshl_b32 s59, s46, 8\n\t"
"s_lshl_b32 s60, s45, 6\n\t"
"s_add_u32 s59, s59, s60\n\t"
"s_lshl_b32 s59, s59, 2\n\t"
"s_add_u32 s66, s59, 33554432\n\t"
"s_lshl_b32 s59, s49, 8\n\t"
"s_and_b32 s60, s45, 1\n\t"
"s_lshl_b32 s60, s60, 6\n\t"
"s_add_u32 s59, s59, s60\n\t"
"s_lshl_b32 s60, s51, 7\n\t"
"s_add_u32 s59, s59, s60\n\t"
"s_lshl_b32 s59, s59, 2\n\t"
"s_add_u32 s77, s59, 33554432\n\t"
"s_lshl_b32 s59, s49, 3\n\t"
"s_add_u32 s59, s59, s50\n\t"
"s_lshl_b32 s59, s59, 2\n\t"
"s_xor_b32 s60, s48, 1\n\t"
"s_add_u32 s60, s60, s59\n\t"
"s_mul_i32 s60, s60, 3\n\t"
"s_add_u32 s60, s60, 0\n\t"
"s_lshl_b32 s60, s60, 14\n\t"
"s_add_u32 s84, s60, 20971520\n\t"
"s_xor_b32 s60, s48, 2\n\t"
"s_add_u32 s60, s60, s59\n\t"
"s_mul_i32 s60, s60, 3\n\t"
"s_add_u32 s60, s60, 1\n\t"
"s_lshl_b32 s60, s60, 14\n\t"
"s_add_u32 s85, s60, 20971520\n\t"
"s_xor_b32 s60, s48, 3\n\t"
"s_add_u32 s60, s60, s59\n\t"
"s_mul_i32 s60, s60, 3\n\t"
"s_add_u32 s60, s60, 2\n\t"
"s_lshl_b32 s60, s60, 14\n\t"
"s_add_u32 s86, s60, 20971520\n\t"
"s_add_u32 s60, s59, s48\n\t"
"s_mul_i32 s87, s60, 49152\n\t"
"s_add_u32 s87, s87, 20971520\n\t"
"s_lshl_b32 s88, s59, 9\n\t"
"s_lshl_b32 s60, s45, 7\n\t"
"s_add_u32 s88, s88, s60\n\t"
"s_add_u32 s88, s88, 33816576\n\t"
"s_lshl_b32 s60, s48, 9\n\t"
"s_add_u32 s89, s88, s60\n\t"
"s_lshl_b32 s59, s49, 8\n\t"
"s_lshl_b32 s60, s51, 7\n\t"
"s_add_u32 s59, s59, s60\n\t"
"s_and_b32 s60, s45, 1\n\t"
"s_lshl_b32 s60, s60, 6\n\t"
"s_add_u32 s59, s59, s60\n\t"
"s_lshl_b32 s59, s59, 11\n\t"
"s_lshl_b32 s60, s50, 8\n\t"
"s_add_u32 s59, s59, s60\n\t"
"s_lshl_b32 s60, s52, 7\n\t"
"s_add_u32 s59, s59, s60\n\t"
"s_lshr_b32 s60, s45, 1\n\t"
"s_lshl_b32 s60, s60, 6\n\t"
"s_add_u32 s90, s59, s60\n\t"
"s_waitcnt lgkmcnt(0)\n\t"
"s_mul_i32 s53, s54, 0x9E3779B1\n\t"
"s_lshr_b32 s61, s53, 15\n\t"
"s_xor_b32 s53, s53, s61\n\t"
"s_mul_i32 s53, s53, 0x85EBCA6B\n\t"
"s_xor_b32 s53, s53, 0x5bd1e995\n\t"
"s_lshl_b32 s68, s47, 3\n\t"
"s_add_u32 s68, s68, s46\n\t"
"s_lshl_b32 s68, s68, 15\n\t"
"s_add_u32 s69, s68, 4096\n\t"
"s_lshl_b32 s65, s47, 2\n\t"
"s_add_u32 s65, s65, s45\n\t"
"s_lshl_b32 s65, s65, 3\n\t"
"s_add_u32 s65, s65, s46\n\t"
"s_lshl_b32 s65, s65, 7\n\t"
"s_add_u32 s65, s65, 33685504\n\t"
"s_mov_b32 s63, 0x3e8293ee\n\t"
"s_mov_b32 s58, 0\n\t"
"s_mov_b32 s57, 53248\n\t"
"s_mov_b32 s61, 53248\n\t"
"s_mov_b32 s62, 4294860800\n\t"
"v_mov_b32 v0, 0\n\t"
"v_mov_b32 v1, 0\n\t"
"v_mov_b32 v2, 0\n\t"
"v_mov_b32 v3, 0\n\t"
"v_mov_b32 v4, 0\n\t"
"v_mov_b32 v5, 0\n\t"
"v_mov_b32 v6, 0\n\t"
"v_mov_b32 v7, 0\n\t"
"v_mov_b32 v8, 0\n\t"
"v_mov_b32 v9, 0\n\t"
"v_mov_b32 v10, 0\n\t"
"v_mov_b32 v11, 0\n\t"
"v_mov_b32 v12, 0\n\t"
"v_mov_b32 v13, 0\n\t"
"v_mov_b32 v14, 0\n\t"
"v_mov_b32 v15, 0\n\t"
"v_mov_b32 v16, 0\n\t"
"v_mov_b32 v17, 0\n\t"
"v_mov_b32 v18, 0\n\t"
"v_mov_b32 v19, 0\n\t"
"v_mov_b32 v20, 0\n\t"
"v_mov_b32 v21, 0\n\t"
"v_mov_b32 v22, 0\n\t"
"v_mov_b32 v23, 0\n\t"
"v_mov_b32 v24, 0\n\t"
"v_mov_b32 v25, 0\n\t"
"v_mov_b32 v26, 0\n\t"
"v_mov_b32 v27, 0\n\t"
"v_mov_b32 v28, 0\n\t"
"v_mov_b32 v29, 0\n\t"
"v_mov_b32 v30, 0\n\t"
"v_mov_b32 v31, 0\n\t"
"v_mov_b32 v32, 0\n\t"
"v_mov_b32 v33, 0\n\t"
"v_mov_b32 v34, 0\n\t"
"v_mov_b32 v35, 0\n\t"
"v_mov_b32 v36, 0\n\t"
"v_mov_b32 v37, 0\n\t"
"v_mov_b32 v38, 0\n\t"
"v_mov_b32 v39, 0\n\t"
"v_mov_b32 v40, 0\n\t"
"v_mov_b32 v41, 0\n\t"
"v_mov_b32 v42, 0\n\t"
"v_mov_b32 v43, 0\n\t"
"v_mov_b32 v44, 0\n\t"
"v_mov_b32 v45, 0\n\t"
"v_mov_b32 v46, 0\n\t"
"v_mov_b32 v47, 0\n\t"
"v_mov_b32 v48, 0\n\t"
"v_mov_b32 v49, 0\n\t"
"v_mov_b32 v50, 0\n\t"
"v_mov_b32 v51, 0\n\t"
"v_mov_b32 v52, 0\n\t"
"v_mov_b32 v53, 0\n\t"
"v_mov_b32 v54, 0\n\t"
"v_mov_b32 v55, 0\n\t"
"v_mov_b32 v56, 0\n\t"
"v_mov_b32 v57, 0\n\t"
"v_mov_b32 v58, 0\n\t"
"v_mov_b32 v59, 0\n\t"
"v_mov_b32 v60, 0\n\t"
"v_mov_b32 v61, 0\n\t"
"v_mov_b32 v62, 0\n\t"
"v_mov_b32 v63, 0\n\t"
"v_mov_b32 v64, 0\n\t"
"v_mov_b32 v65, 0\n\t"
"v_mov_b32 v66, 0\n\t"
"v_mov_b32 v67, 0\n\t"
"v_mov_b32 v68, 0\n\t"
"v_mov_b32 v69, 0\n\t"
"v_mov_b32 v70, 0\n\t"
"v_mov_b32 v71, 0\n\t"
"v_mov_b32 v72, 0\n\t"
"v_mov_b32 v73, 0\n\t"
"v_mov_b32 v74, 0\n\t"
"v_mov_b32 v75, 0\n\t"
"v_mov_b32 v76, 0\n\t"
"v_mov_b32 v77, 0\n\t"
"v_mov_b32 v78, 0\n\t"
"v_mov_b32 v79, 0\n\t"
"v_mov_b32 v80, 0\n\t"
"v_mov_b32 v81, 0\n\t"
"v_mov_b32 v82, 0\n\t"
"v_mov_b32 v83, 0\n\t"
"v_mov_b32 v84, 0\n\t"
"v_mov_b32 v85, 0\n\t"
"v_mov_b32 v86, 0\n\t"
"v_mov_b32 v87, 0\n\t"
"v_mov_b32 v88, 0\n\t"
"v_mov_b32 v89, 0\n\t"
"v_mov_b32 v90, 0\n\t"
"v_mov_b32 v91, 0\n\t"
"v_mov_b32 v92, 0\n\t"
"v_mov_b32 v93, 0\n\t"
"v_mov_b32 v94, 0\n\t"
"v_mov_b32 v95, 0\n\t"
"v_mov_b32 v96, 0\n\t"
"v_mov_b32 v97, 0\n\t"
"v_mov_b32 v98, 0\n\t"
"v_mov_b32 v99, 0\n\t"
"v_mov_b32 v100, 0\n\t"
"v_mov_b32 v101, 0\n\t"
"v_mov_b32 v102, 0\n\t"
"v_mov_b32 v103, 0\n\t"
"v_mov_b32 v104, 0\n\t"
"v_mov_b32 v105, 0\n\t"
"v_mov_b32 v106, 0\n\t"
"v_mov_b32 v107, 0\n\t"
"v_mov_b32 v108, 0\n\t"
"v_mov_b32 v109, 0\n\t"
"v_mov_b32 v110, 0\n\t"
"v_mov_b32 v111, 0\n\t"
"v_mov_b32 v112, 0\n\t"
"v_mov_b32 v113, 0\n\t"
"v_mov_b32 v114, 0\n\t"
"v_mov_b32 v115, 0\n\t"
"v_mov_b32 v116, 0\n\t"
"v_mov_b32 v117, 0\n\t"
"v_mov_b32 v118, 0\n\t"
"v_mov_b32 v119, 0\n\t"
"v_mov_b32 v120, 0\n\t"
"v_mov_b32 v121, 0\n\t"
"v_mov_b32 v122, 0\n\t"
"v_mov_b32 v123, 0\n\t"
"v_mov_b32 v124, 0\n\t"
"v_mov_b32 v125, 0\n\t"
"v_mov_b32 v126, 0\n\t"
"v_mov_b32 v127, 0\n\t"
"v_mov_b32 v128, 0\n\t"
"v_mov_b32 v129, 0\n\t"
"v_mov_b32 v130, 0\n\t"
"v_mov_b32 v131, 0\n\t"
"v_mov_b32 v132, 0\n\t"
"v_mov_b32 v133, 0\n\t"
"v_mov_b32 v134, 0\n\t"
"v_mov_b32 v135, 0\n\t"
"v_mov_b32 v136, 0\n\t"
"v_mov_b32 v137, 0\n\t"
"v_mov_b32 v138, 0\n\t"
"v_mov_b32 v139, 0\n\t"
"v_mov_b32 v140, 0\n\t"
"v_mov_b32 v141, 0\n\t"
"v_mov_b32 v142, 0\n\t"
"v_mov_b32 v143, 0\n\t"
"v_mov_b32 v144, 0\n\t"
"v_mov_b32 v145, 0\n\t"
"v_mov_b32 v146, 0\n\t"
"v_mov_b32 v147, 0\n\t"
"v_mov_b32 v148, 0\n\t"
"v_mov_b32 v149, 0\n\t"
"v_mov_b32 v150, 0\n\t"
"v_mov_b32 v151, 0\n\t"
"v_mov_b32 v152, 0\n\t"
"v_mov_b32 v153, 0\n\t"
"v_mov_b32 v154, 0\n\t"
"v_mov_b32 v155, 0\n\t"
"v_mov_b32 v156, 0\n\t"
"v_mov_b32 v157, 0\n\t"
"v_mov_b32 v158, 0\n\t"
"v_mov_b32 v159, 0\n\t"
"s_barrier\n\t"
"ds_read_b128 v[160:163], v232 offset:0\n\t"
"ds_read_b128 v[164:167], v232 offset:2048\n\t"
"ds_read_b128 v[168:171], v232 offset:4096\n\t"
"ds_read_b128 v[172:175], v232 offset:6144\n\t"
"ds_read_b64_tr_b16 v[192:193], v234 offset:0\n\t"
"ds_read_b64_tr_b16 v[194:195], v234 offset:256\n\t"
"ds_read_b64_tr_b16 v[196:197], v235 offset:0\n\t"
"ds_read_b64_tr_b16 v[198:199], v235 offset:256\n\t"
"ds_read_b64_tr_b16 v[200:201], v234 offset:1024\n\t"
"ds_read_b64_tr_b16 v[202:203], v234 offset:1280\n\t"
"ds_read_b64_tr_b16 v[204:205], v235 offset:1024\n\t"
"ds_read_b64_tr_b16 v[206:207], v235 offset:1280\n\t"
"ds_read_b64_tr_b16 v[208:209], v234 offset:2048\n\t"
"ds_read_b64_tr_b16 v[210:211], v234 offset:2304\n\t"
"ds_read_b64_tr_b16 v[212:213], v235 offset:2048\n\t"
"ds_read_b64_tr_b16 v[214:215], v235 offset:2304\n\t"
"ds_read_b128 v[216:219], v236 offset:0\n\t"
"ds_read_b128 v[220:223], v236 offset:4096\n\t"
"ds_read_b128 v[224:227], v236 offset:1024\n\t"
"ds_read_b128 v[228:231], v236 offset:5120\n\t"
"s_waitcnt lgkmcnt(0)\n\t"
"s_mov_b32 s56, 5\n\t"
".p2align 6\n\t"
"s_nop 0\n\t"
"s_nop 0\n\t"
"L_g1c_%=:\n\t"
"s_waitcnt lgkmcnt(3)\n\t"
"v_mfma_f32_16x16x32_bf16 v[0:3], v[192:195], v[160:163], v[0:3]\n\t"
"ds_read_b128 v[228:231], v236 offset:5120\n\t"
"v_mfma_f32_16x16x32_bf16 v[16:19], v[196:199], v[160:163], v[16:19]\n\t"
"ds_read_b128 v[176:179], v233 offset:0\n\t"
"v_mfma_f32_16x16x32_bf16 v[20:23], v[196:199], v[164:167], v[20:23]\n\t"
"ds_read_b128 v[180:183], v233 offset:2048\n\t"
"v_mfma_f32_16x16x32_bf16 v[4:7], v[192:195], v[164:167], v[4:7]\n\t"
"ds_read_b128 v[184:187], v233 offset:4096\n\t"
"v_mfma_f32_16x16x32_bf16 v[8:11], v[192:195], v[168:171], v[8:11]\n\t"
"ds_read_b128 v[188:191], v233 offset:6144\n\t"
"v_mfma_f32_16x16x32_bf16 v[24:27], v[196:199], v[168:171], v[24:27]\n\t"
"v_mfma_f32_16x16x32_bf16 v[28:31], v[196:199], v[172:175], v[28:31]\n\t"
"v_mfma_f32_16x16x32_bf16 v[12:15], v[192:195], v[172:175], v[12:15]\n\t"
"v_mfma_f32_16x16x32_bf16 v[44:47], v[200:203], v[172:175], v[44:47]\n\t"
"ds_read_b64_tr_b16 v[192:193], v234 offset:6144\n\t"
"v_mfma_f32_16x16x32_bf16 v[60:63], v[204:207], v[172:175], v[60:63]\n\t"
"ds_read_b64_tr_b16 v[194:195], v234 offset:6400\n\t"
"v_mfma_f32_16x16x32_bf16 v[56:59], v[204:207], v[168:171], v[56:59]\n\t"
"ds_read_b64_tr_b16 v[196:197], v235 offset:6144\n\t"
"v_mfma_f32_16x16x32_bf16 v[40:43], v[200:203], v[168:171], v[40:43]\n\t"
"ds_read_b64_tr_b16 v[198:199], v235 offset:6400\n\t"
"v_mfma_f32_16x16x32_bf16 v[36:39], v[200:203], v[164:167], v[36:39]\n\t"
"v_mfma_f32_16x16x32_bf16 v[52:55], v[204:207], v[164:167], v[52:55]\n\t"
"v_mfma_f32_16x16x32_bf16 v[48:51], v[204:207], v[160:163], v[48:51]\n\t"
"v_mfma_f32_16x16x32_bf16 v[32:35], v[200:203], v[160:163], v[32:35]\n\t"
"v_mfma_f32_16x16x32_bf16 v[64:67], v[160:163], v[208:211], v[64:67]\n\t"
"ds_read_b64_tr_b16 v[200:201], v234 offset:7168\n\t"
"v_mfma_f32_16x16x32_bf16 v[80:83], v[160:163], v[212:215], v[80:83]\n\t"
"ds_read_b64_tr_b16 v[202:203], v234 offset:7424\n\t"
"v_mfma_f32_16x16x32_bf16 v[84:87], v[164:167], v[212:215], v[84:87]\n\t"
"ds_read_b64_tr_b16 v[204:205], v235 offset:7168\n\t"
"v_mfma_f32_16x16x32_bf16 v[68:71], v[164:167], v[208:211], v[68:71]\n\t"
"ds_read_b64_tr_b16 v[206:207], v235 offset:7424\n\t"
"v_mfma_f32_16x16x32_bf16 v[72:75], v[168:171], v[208:211], v[72:75]\n\t"
"v_mfma_f32_16x16x32_bf16 v[88:91], v[168:171], v[212:215], v[88:91]\n\t"
"v_mfma_f32_16x16x32_bf16 v[92:95], v[172:175], v[212:215], v[92:95]\n\t"
"v_mfma_f32_16x16x32_bf16 v[76:79], v[172:175], v[208:211], v[76:79]\n\t"
"s_waitcnt lgkmcnt(4)\n\t"
"v_mfma_f32_16x16x32_bf16 v[108:111], v[172:175], v[216:219], v[108:111]\n\t"
"ds_read_b64_tr_b16 v[208:209], v234 offset:8192\n\t"
"v_mfma_f32_16x16x32_bf16 v[124:127], v[172:175], v[220:223], v[124:127]\n\t"
"ds_read_b64_tr_b16 v[210:211], v234 offset:8448\n\t"
"v_mfma_f32_16x16x32_bf16 v[120:123], v[168:171], v[220:223], v[120:123]\n\t"
"ds_read_b64_tr_b16 v[212:213], v235 offset:8192\n\t"
"v_mfma_f32_16x16x32_bf16 v[104:107], v[168:171], v[216:219], v[104:107]\n\t"
"ds_read_b64_tr_b16 v[214:215], v235 offset:8448\n\t"
"v_mfma_f32_16x16x32_bf16 v[100:103], v[164:167], v[216:219], v[100:103]\n\t"
"v_mfma_f32_16x16x32_bf16 v[116:119], v[164:167], v[220:223], v[116:119]\n\t"
"v_mfma_f32_16x16x32_bf16 v[112:115], v[160:163], v[220:223], v[112:115]\n\t"
"v_mfma_f32_16x16x32_bf16 v[96:99], v[160:163], v[216:219], v[96:99]\n\t"
"v_mfma_f32_16x16x32_bf16 v[128:131], v[160:163], v[224:227], v[128:131]\n\t"
"ds_read_b128 v[216:219], v237 offset:0\n\t"
"v_mfma_f32_16x16x32_bf16 v[144:147], v[160:163], v[228:231], v[144:147]\n\t"
"ds_read_b128 v[220:223], v237 offset:4096\n\t"
"v_mfma_f32_16x16x32_bf16 v[148:151], v[164:167], v[228:231], v[148:151]\n\t"
"v_mfma_f32_16x16x32_bf16 v[132:135], v[164:167], v[224:227], v[132:135]\n\t"
"v_mfma_f32_16x16x32_bf16 v[136:139], v[168:171], v[224:227], v[136:139]\n\t"
"v_mfma_f32_16x16x32_bf16 v[152:155], v[168:171], v[228:231], v[152:155]\n\t"
"v_mfma_f32_16x16x32_bf16 v[156:159], v[172:175], v[228:231], v[156:159]\n\t"
"v_mfma_f32_16x16x32_bf16 v[140:143], v[172:175], v[224:227], v[140:143]\n\t"
"ds_read_b128 v[224:227], v237 offset:1024\n\t"
"v_mfma_f32_16x16x32_bf16 v[0:3], v[192:195], v[176:179], v[0:3]\n\t"
"ds_read_b128 v[228:231], v237 offset:5120\n\t"
"v_mfma_f32_16x16x32_bf16 v[16:19], v[196:199], v[176:179], v[16:19]\n\t"
"v_mfma_f32_16x16x32_bf16 v[20:23], v[196:199], v[180:183], v[20:23]\n\t"
"v_mfma_f32_16x16x32_bf16 v[4:7], v[192:195], v[180:183], v[4:7]\n\t"
"v_mfma_f32_16x16x32_bf16 v[8:11], v[192:195], v[184:187], v[8:11]\n\t"
"v_mfma_f32_16x16x32_bf16 v[24:27], v[196:199], v[184:187], v[24:27]\n\t"
"v_mfma_f32_16x16x32_bf16 v[28:31], v[196:199], v[188:191], v[28:31]\n\t"
"v_mfma_f32_16x16x32_bf16 v[12:15], v[192:195], v[188:191], v[12:15]\n\t"
"s_waitcnt lgkmcnt(0)\n\t"
"s_barrier\n\t"
"v_mfma_f32_16x16x32_bf16 v[44:47], v[200:203], v[188:191], v[44:47]\n\t"
"ds_read_b128 v[160:163], v232 offset:53248\n\t"
"v_mfma_f32_16x16x32_bf16 v[60:63], v[204:207], v[188:191], v[60:63]\n\t"
"ds_read_b128 v[164:167], v232 offset:55296\n\t"
"v_mfma_f32_16x16x32_bf16 v[56:59], v[204:207], v[184:187], v[56:59]\n\t"
"ds_read_b128 v[168:171], v232 offset:57344\n\t"
"v_mfma_f32_16x16x32_bf16 v[40:43], v[200:203], v[184:187], v[40:43]\n\t"
"ds_read_b128 v[172:175], v232 offset:59392\n\t"
"v_mfma_f32_16x16x32_bf16 v[36:39], v[200:203], v[180:183], v[36:39]\n\t"
"ds_read_b64_tr_b16 v[192:193], v234 offset:53248\n\t"
"v_mfma_f32_16x16x32_bf16 v[52:55], v[204:207], v[180:183], v[52:55]\n\t"
"ds_read_b64_tr_b16 v[194:195], v234 offset:53504\n\t"
"v_mfma_f32_16x16x32_bf16 v[48:51], v[204:207], v[176:179], v[48:51]\n\t"
"ds_read_b64_tr_b16 v[196:197], v235 offset:53248\n\t"
"v_mfma_f32_16x16x32_bf16 v[32:35], v[200:203], v[176:179], v[32:35]\n\t"
"ds_read_b64_tr_b16 v[198:199], v235 offset:53504\n\t"
"v_mfma_f32_16x16x32_bf16 v[64:67], v[176:179], v[208:211], v[64:67]\n\t"
"ds_read_b64_tr_b16 v[200:201], v234 offset:54272\n\t"
"v_mfma_f32_16x16x32_bf16 v[80:83], v[176:179], v[212:215], v[80:83]\n\t"
"ds_read_b64_tr_b16 v[202:203], v234 offset:54528\n\t"
"v_mfma_f32_16x16x32_bf16 v[84:87], v[180:183], v[212:215], v[84:87]\n\t"
"ds_read_b64_tr_b16 v[204:205], v235 offset:54272\n\t"
"v_mfma_f32_16x16x32_bf16 v[68:71], v[180:183], v[208:211], v[68:71]\n\t"
"ds_read_b64_tr_b16 v[206:207], v235 offset:54528\n\t"
"v_mfma_f32_16x16x32_bf16 v[72:75], v[184:187], v[208:211], v[72:75]\n\t"
"v_mfma_f32_16x16x32_bf16 v[88:91], v[184:187], v[212:215], v[88:91]\n\t"
"v_mfma_f32_16x16x32_bf16 v[92:95], v[188:191], v[212:215], v[92:95]\n\t"
"v_mfma_f32_16x16x32_bf16 v[76:79], v[188:191], v[208:211], v[76:79]\n\t"
"v_mfma_f32_16x16x32_bf16 v[108:111], v[188:191], v[216:219], v[108:111]\n\t"
"ds_read_b64_tr_b16 v[208:209], v234 offset:55296\n\t"
"v_mfma_f32_16x16x32_bf16 v[124:127], v[188:191], v[220:223], v[124:127]\n\t"
"ds_read_b64_tr_b16 v[210:211], v234 offset:55552\n\t"
"v_mfma_f32_16x16x32_bf16 v[120:123], v[184:187], v[220:223], v[120:123]\n\t"
"ds_read_b64_tr_b16 v[212:213], v235 offset:55296\n\t"
"v_mfma_f32_16x16x32_bf16 v[104:107], v[184:187], v[216:219], v[104:107]\n\t"
"ds_read_b64_tr_b16 v[214:215], v235 offset:55552\n\t"
"v_mfma_f32_16x16x32_bf16 v[100:103], v[180:183], v[216:219], v[100:103]\n\t"
"v_mfma_f32_16x16x32_bf16 v[116:119], v[180:183], v[220:223], v[116:119]\n\t"
"v_mfma_f32_16x16x32_bf16 v[112:115], v[176:179], v[220:223], v[112:115]\n\t"
"v_mfma_f32_16x16x32_bf16 v[96:99], v[176:179], v[216:219], v[96:99]\n\t"
"v_mfma_f32_16x16x32_bf16 v[128:131], v[176:179], v[224:227], v[128:131]\n\t"
"ds_read_b128 v[216:219], v236 offset:53248\n\t"
"v_mfma_f32_16x16x32_bf16 v[144:147], v[176:179], v[228:231], v[144:147]\n\t"
"ds_read_b128 v[220:223], v236 offset:57344\n\t"
"v_mfma_f32_16x16x32_bf16 v[148:151], v[180:183], v[228:231], v[148:151]\n\t"
"v_mfma_f32_16x16x32_bf16 v[132:135], v[180:183], v[224:227], v[132:135]\n\t"
"v_mfma_f32_16x16x32_bf16 v[136:139], v[184:187], v[224:227], v[136:139]\n\t"
"v_mfma_f32_16x16x32_bf16 v[152:155], v[184:187], v[228:231], v[152:155]\n\t"
"v_mfma_f32_16x16x32_bf16 v[156:159], v[188:191], v[228:231], v[156:159]\n\t"
"v_mfma_f32_16x16x32_bf16 v[140:143], v[188:191], v[224:227], v[140:143]\n\t"
"ds_read_b128 v[224:227], v236 offset:54272\n\t"
"s_waitcnt lgkmcnt(3)\n\t"
"v_mfma_f32_16x16x32_bf16 v[0:3], v[192:195], v[160:163], v[0:3]\n\t"
"ds_read_b128 v[228:231], v236 offset:58368\n\t"
"v_mfma_f32_16x16x32_bf16 v[16:19], v[196:199], v[160:163], v[16:19]\n\t"
"ds_read_b128 v[176:179], v233 offset:53248\n\t"
"v_mfma_f32_16x16x32_bf16 v[20:23], v[196:199], v[164:167], v[20:23]\n\t"
"ds_read_b128 v[180:183], v233 offset:55296\n\t"
"v_mfma_f32_16x16x32_bf16 v[4:7], v[192:195], v[164:167], v[4:7]\n\t"
"ds_read_b128 v[184:187], v233 offset:57344\n\t"
"v_mfma_f32_16x16x32_bf16 v[8:11], v[192:195], v[168:171], v[8:11]\n\t"
"ds_read_b128 v[188:191], v233 offset:59392\n\t"
"v_mfma_f32_16x16x32_bf16 v[24:27], v[196:199], v[168:171], v[24:27]\n\t"
"v_mfma_f32_16x16x32_bf16 v[28:31], v[196:199], v[172:175], v[28:31]\n\t"
"v_mfma_f32_16x16x32_bf16 v[12:15], v[192:195], v[172:175], v[12:15]\n\t"
"v_mfma_f32_16x16x32_bf16 v[44:47], v[200:203], v[172:175], v[44:47]\n\t"
"ds_read_b64_tr_b16 v[192:193], v234 offset:59392\n\t"
"v_mfma_f32_16x16x32_bf16 v[60:63], v[204:207], v[172:175], v[60:63]\n\t"
"ds_read_b64_tr_b16 v[194:195], v234 offset:59648\n\t"
"v_mfma_f32_16x16x32_bf16 v[56:59], v[204:207], v[168:171], v[56:59]\n\t"
"ds_read_b64_tr_b16 v[196:197], v235 offset:59392\n\t"
"v_mfma_f32_16x16x32_bf16 v[40:43], v[200:203], v[168:171], v[40:43]\n\t"
"ds_read_b64_tr_b16 v[198:199], v235 offset:59648\n\t"
"v_mfma_f32_16x16x32_bf16 v[36:39], v[200:203], v[164:167], v[36:39]\n\t"
"v_mfma_f32_16x16x32_bf16 v[52:55], v[204:207], v[164:167], v[52:55]\n\t"
"v_mfma_f32_16x16x32_bf16 v[48:51], v[204:207], v[160:163], v[48:51]\n\t"
"v_mfma_f32_16x16x32_bf16 v[32:35], v[200:203], v[160:163], v[32:35]\n\t"
"v_mfma_f32_16x16x32_bf16 v[64:67], v[160:163], v[208:211], v[64:67]\n\t"
"ds_read_b64_tr_b16 v[200:201], v234 offset:60416\n\t"
"v_mfma_f32_16x16x32_bf16 v[80:83], v[160:163], v[212:215], v[80:83]\n\t"
"ds_read_b64_tr_b16 v[202:203], v234 offset:60672\n\t"
"v_mfma_f32_16x16x32_bf16 v[84:87], v[164:167], v[212:215], v[84:87]\n\t"
"ds_read_b64_tr_b16 v[204:205], v235 offset:60416\n\t"
"v_mfma_f32_16x16x32_bf16 v[68:71], v[164:167], v[208:211], v[68:71]\n\t"
"ds_read_b64_tr_b16 v[206:207], v235 offset:60672\n\t"
"v_mfma_f32_16x16x32_bf16 v[72:75], v[168:171], v[208:211], v[72:75]\n\t"
"v_mfma_f32_16x16x32_bf16 v[88:91], v[168:171], v[212:215], v[88:91]\n\t"
"v_mfma_f32_16x16x32_bf16 v[92:95], v[172:175], v[212:215], v[92:95]\n\t"
"v_mfma_f32_16x16x32_bf16 v[76:79], v[172:175], v[208:211], v[76:79]\n\t"
"s_waitcnt lgkmcnt(4)\n\t"
"v_mfma_f32_16x16x32_bf16 v[108:111], v[172:175], v[216:219], v[108:111]\n\t"
"ds_read_b64_tr_b16 v[208:209], v234 offset:61440\n\t"
"v_mfma_f32_16x16x32_bf16 v[124:127], v[172:175], v[220:223], v[124:127]\n\t"
"ds_read_b64_tr_b16 v[210:211], v234 offset:61696\n\t"
"v_mfma_f32_16x16x32_bf16 v[120:123], v[168:171], v[220:223], v[120:123]\n\t"
"ds_read_b64_tr_b16 v[212:213], v235 offset:61440\n\t"
"v_mfma_f32_16x16x32_bf16 v[104:107], v[168:171], v[216:219], v[104:107]\n\t"
"ds_read_b64_tr_b16 v[214:215], v235 offset:61696\n\t"
"v_mfma_f32_16x16x32_bf16 v[100:103], v[164:167], v[216:219], v[100:103]\n\t"
"v_mfma_f32_16x16x32_bf16 v[116:119], v[164:167], v[220:223], v[116:119]\n\t"
"v_mfma_f32_16x16x32_bf16 v[112:115], v[160:163], v[220:223], v[112:115]\n\t"
"v_mfma_f32_16x16x32_bf16 v[96:99], v[160:163], v[216:219], v[96:99]\n\t"
"v_mfma_f32_16x16x32_bf16 v[128:131], v[160:163], v[224:227], v[128:131]\n\t"
"ds_read_b128 v[216:219], v237 offset:53248\n\t"
"v_mfma_f32_16x16x32_bf16 v[144:147], v[160:163], v[228:231], v[144:147]\n\t"
"ds_read_b128 v[220:223], v237 offset:57344\n\t"
"v_mfma_f32_16x16x32_bf16 v[148:151], v[164:167], v[228:231], v[148:151]\n\t"
"v_mfma_f32_16x16x32_bf16 v[132:135], v[164:167], v[224:227], v[132:135]\n\t"
"v_mfma_f32_16x16x32_bf16 v[136:139], v[168:171], v[224:227], v[136:139]\n\t"
"v_mfma_f32_16x16x32_bf16 v[152:155], v[168:171], v[228:231], v[152:155]\n\t"
"v_mfma_f32_16x16x32_bf16 v[156:159], v[172:175], v[228:231], v[156:159]\n\t"
"v_mfma_f32_16x16x32_bf16 v[140:143], v[172:175], v[224:227], v[140:143]\n\t"
"ds_read_b128 v[224:227], v237 offset:54272\n\t"
"v_mfma_f32_16x16x32_bf16 v[0:3], v[192:195], v[176:179], v[0:3]\n\t"
"ds_read_b128 v[228:231], v237 offset:58368\n\t"
"v_mfma_f32_16x16x32_bf16 v[16:19], v[196:199], v[176:179], v[16:19]\n\t"
"v_mfma_f32_16x16x32_bf16 v[20:23], v[196:199], v[180:183], v[20:23]\n\t"
"v_mfma_f32_16x16x32_bf16 v[4:7], v[192:195], v[180:183], v[4:7]\n\t"
"v_mfma_f32_16x16x32_bf16 v[8:11], v[192:195], v[184:187], v[8:11]\n\t"
"v_mfma_f32_16x16x32_bf16 v[24:27], v[196:199], v[184:187], v[24:27]\n\t"
"v_mfma_f32_16x16x32_bf16 v[28:31], v[196:199], v[188:191], v[28:31]\n\t"
"v_mfma_f32_16x16x32_bf16 v[12:15], v[192:195], v[188:191], v[12:15]\n\t"
"s_waitcnt lgkmcnt(0)\n\t"
"s_barrier\n\t"
"v_mfma_f32_16x16x32_bf16 v[44:47], v[200:203], v[188:191], v[44:47]\n\t"
"v_add_u32 v232, 0xd000, v232\n\t"
"v_mfma_f32_16x16x32_bf16 v[60:63], v[204:207], v[188:191], v[60:63]\n\t"
"v_add_u32 v234, 0xd000, v234\n\t"
"v_mfma_f32_16x16x32_bf16 v[56:59], v[204:207], v[184:187], v[56:59]\n\t"
"v_add_u32 v235, 0xd000, v235\n\t"
"v_mfma_f32_16x16x32_bf16 v[40:43], v[200:203], v[184:187], v[40:43]\n\t"
"ds_read_b128 v[160:163], v232 offset:53248\n\t"
"v_mfma_f32_16x16x32_bf16 v[36:39], v[200:203], v[180:183], v[36:39]\n\t"
"ds_read_b128 v[164:167], v232 offset:55296\n\t"
"v_mfma_f32_16x16x32_bf16 v[52:55], v[204:207], v[180:183], v[52:55]\n\t"
"ds_read_b128 v[168:171], v232 offset:57344\n\t"
"v_mfma_f32_16x16x32_bf16 v[48:51], v[204:207], v[176:179], v[48:51]\n\t"
"ds_read_b128 v[172:175], v232 offset:59392\n\t"
"v_mfma_f32_16x16x32_bf16 v[32:35], v[200:203], v[176:179], v[32:35]\n\t"
"v_add_u32 v236, 0xd000, v236\n\t"
"v_mfma_f32_16x16x32_bf16 v[64:67], v[176:179], v[208:211], v[64:67]\n\t"
"ds_read_b64_tr_b16 v[192:193], v234 offset:53248\n\t"
"v_mfma_f32_16x16x32_bf16 v[80:83], v[176:179], v[212:215], v[80:83]\n\t"
"ds_read_b64_tr_b16 v[194:195], v234 offset:53504\n\t"
"v_mfma_f32_16x16x32_bf16 v[84:87], v[180:183], v[212:215], v[84:87]\n\t"
"v_add_u32 v233, 0xd000, v233\n\t"
"v_mfma_f32_16x16x32_bf16 v[68:71], v[180:183], v[208:211], v[68:71]\n\t"
"ds_read_b64_tr_b16 v[196:197], v235 offset:53248\n\t"
"v_mfma_f32_16x16x32_bf16 v[72:75], v[184:187], v[208:211], v[72:75]\n\t"
"ds_read_b64_tr_b16 v[198:199], v235 offset:53504\n\t"
"v_mfma_f32_16x16x32_bf16 v[88:91], v[184:187], v[212:215], v[88:91]\n\t"
"v_add_u32 v237, 0xd000, v237\n\t"
"v_mfma_f32_16x16x32_bf16 v[92:95], v[188:191], v[212:215], v[92:95]\n\t"
"ds_read_b64_tr_b16 v[200:201], v234 offset:54272\n\t"
"v_mfma_f32_16x16x32_bf16 v[76:79], v[188:191], v[208:211], v[76:79]\n\t"
"ds_read_b64_tr_b16 v[202:203], v234 offset:54528\n\t"
"v_mfma_f32_16x16x32_bf16 v[108:111], v[188:191], v[216:219], v[108:111]\n\t"
"ds_read_b64_tr_b16 v[204:205], v235 offset:54272\n\t"
"v_mfma_f32_16x16x32_bf16 v[124:127], v[188:191], v[220:223], v[124:127]\n\t"
"ds_read_b64_tr_b16 v[206:207], v235 offset:54528\n\t"
"v_mfma_f32_16x16x32_bf16 v[120:123], v[184:187], v[220:223], v[120:123]\n\t"
"ds_read_b64_tr_b16 v[208:209], v234 offset:55296\n\t"
"v_mfma_f32_16x16x32_bf16 v[104:107], v[184:187], v[216:219], v[104:107]\n\t"
"ds_read_b64_tr_b16 v[210:211], v234 offset:55552\n\t"
"v_mfma_f32_16x16x32_bf16 v[100:103], v[180:183], v[216:219], v[100:103]\n\t"
"ds_read_b64_tr_b16 v[212:213], v235 offset:55296\n\t"
"v_mfma_f32_16x16x32_bf16 v[116:119], v[180:183], v[220:223], v[116:119]\n\t"
"ds_read_b64_tr_b16 v[214:215], v235 offset:55552\n\t"
"v_mfma_f32_16x16x32_bf16 v[112:115], v[176:179], v[220:223], v[112:115]\n\t"
"v_mfma_f32_16x16x32_bf16 v[96:99], v[176:179], v[216:219], v[96:99]\n\t"
"v_mfma_f32_16x16x32_bf16 v[128:131], v[176:179], v[224:227], v[128:131]\n\t"
"ds_read_b128 v[216:219], v236 offset:53248\n\t"
"v_mfma_f32_16x16x32_bf16 v[144:147], v[176:179], v[228:231], v[144:147]\n\t"
"ds_read_b128 v[220:223], v236 offset:57344\n\t"
"v_mfma_f32_16x16x32_bf16 v[148:151], v[180:183], v[228:231], v[148:151]\n\t"
"v_mfma_f32_16x16x32_bf16 v[132:135], v[180:183], v[224:227], v[132:135]\n\t"
"v_mfma_f32_16x16x32_bf16 v[136:139], v[184:187], v[224:227], v[136:139]\n\t"
"v_mfma_f32_16x16x32_bf16 v[152:155], v[184:187], v[228:231], v[152:155]\n\t"
"v_mfma_f32_16x16x32_bf16 v[156:159], v[188:191], v[228:231], v[156:159]\n\t"
"v_mfma_f32_16x16x32_bf16 v[140:143], v[188:191], v[224:227], v[140:143]\n\t"
"ds_read_b128 v[224:227], v236 offset:54272\n\t"
"s_waitcnt lgkmcnt(3)\n\t"
"v_mfma_f32_16x16x32_bf16 v[0:3], v[192:195], v[160:163], v[0:3]\n\t"
"ds_read_b128 v[228:231], v236 offset:58368\n\t"
"v_mfma_f32_16x16x32_bf16 v[16:19], v[196:199], v[160:163], v[16:19]\n\t"
"ds_read_b128 v[176:179], v233 offset:53248\n\t"
"v_mfma_f32_16x16x32_bf16 v[20:23], v[196:199], v[164:167], v[20:23]\n\t"
"ds_read_b128 v[180:183], v233 offset:55296\n\t"
"v_mfma_f32_16x16x32_bf16 v[4:7], v[192:195], v[164:167], v[4:7]\n\t"
"ds_read_b128 v[184:187], v233 offset:57344\n\t"
"v_mfma_f32_16x16x32_bf16 v[8:11], v[192:195], v[168:171], v[8:11]\n\t"
"ds_read_b128 v[188:191], v233 offset:59392\n\t"
"v_mfma_f32_16x16x32_bf16 v[24:27], v[196:199], v[168:171], v[24:27]\n\t"
"v_mfma_f32_16x16x32_bf16 v[28:31], v[196:199], v[172:175], v[28:31]\n\t"
"v_mfma_f32_16x16x32_bf16 v[12:15], v[192:195], v[172:175], v[12:15]\n\t"
"v_mfma_f32_16x16x32_bf16 v[44:47], v[200:203], v[172:175], v[44:47]\n\t"
"ds_read_b64_tr_b16 v[192:193], v234 offset:59392\n\t"
"v_mfma_f32_16x16x32_bf16 v[60:63], v[204:207], v[172:175], v[60:63]\n\t"
"ds_read_b64_tr_b16 v[194:195], v234 offset:59648\n\t"
"v_mfma_f32_16x16x32_bf16 v[56:59], v[204:207], v[168:171], v[56:59]\n\t"
"ds_read_b64_tr_b16 v[196:197], v235 offset:59392\n\t"
"v_mfma_f32_16x16x32_bf16 v[40:43], v[200:203], v[168:171], v[40:43]\n\t"
"ds_read_b64_tr_b16 v[198:199], v235 offset:59648\n\t"
"v_mfma_f32_16x16x32_bf16 v[36:39], v[200:203], v[164:167], v[36:39]\n\t"
"v_mfma_f32_16x16x32_bf16 v[52:55], v[204:207], v[164:167], v[52:55]\n\t"
"v_mfma_f32_16x16x32_bf16 v[48:51], v[204:207], v[160:163], v[48:51]\n\t"
"v_mfma_f32_16x16x32_bf16 v[32:35], v[200:203], v[160:163], v[32:35]\n\t"
"v_mfma_f32_16x16x32_bf16 v[64:67], v[160:163], v[208:211], v[64:67]\n\t"
"ds_read_b64_tr_b16 v[200:201], v234 offset:60416\n\t"
"v_mfma_f32_16x16x32_bf16 v[80:83], v[160:163], v[212:215], v[80:83]\n\t"
"ds_read_b64_tr_b16 v[202:203], v234 offset:60672\n\t"
"v_mfma_f32_16x16x32_bf16 v[84:87], v[164:167], v[212:215], v[84:87]\n\t"
"ds_read_b64_tr_b16 v[204:205], v235 offset:60416\n\t"
"v_mfma_f32_16x16x32_bf16 v[68:71], v[164:167], v[208:211], v[68:71]\n\t"
"ds_read_b64_tr_b16 v[206:207], v235 offset:60672\n\t"
"v_mfma_f32_16x16x32_bf16 v[72:75], v[168:171], v[208:211], v[72:75]\n\t"
"v_mfma_f32_16x16x32_bf16 v[88:91], v[168:171], v[212:215], v[88:91]\n\t"
"v_mfma_f32_16x16x32_bf16 v[92:95], v[172:175], v[212:215], v[92:95]\n\t"
"v_mfma_f32_16x16x32_bf16 v[76:79], v[172:175], v[208:211], v[76:79]\n\t"
"s_waitcnt lgkmcnt(4)\n\t"
"v_mfma_f32_16x16x32_bf16 v[108:111], v[172:175], v[216:219], v[108:111]\n\t"
"ds_read_b64_tr_b16 v[208:209], v234 offset:61440\n\t"
"v_mfma_f32_16x16x32_bf16 v[124:127], v[172:175], v[220:223], v[124:127]\n\t"
"ds_read_b64_tr_b16 v[210:211], v234 offset:61696\n\t"
"v_mfma_f32_16x16x32_bf16 v[120:123], v[168:171], v[220:223], v[120:123]\n\t"
"ds_read_b64_tr_b16 v[212:213], v235 offset:61440\n\t"
"v_mfma_f32_16x16x32_bf16 v[104:107], v[168:171], v[216:219], v[104:107]\n\t"
"ds_read_b64_tr_b16 v[214:215], v235 offset:61696\n\t"
"v_mfma_f32_16x16x32_bf16 v[100:103], v[164:167], v[216:219], v[100:103]\n\t"
"v_mfma_f32_16x16x32_bf16 v[116:119], v[164:167], v[220:223], v[116:119]\n\t"
"v_mfma_f32_16x16x32_bf16 v[112:115], v[160:163], v[220:223], v[112:115]\n\t"
"v_mfma_f32_16x16x32_bf16 v[96:99], v[160:163], v[216:219], v[96:99]\n\t"
"v_mfma_f32_16x16x32_bf16 v[128:131], v[160:163], v[224:227], v[128:131]\n\t"
"ds_read_b128 v[216:219], v237 offset:53248\n\t"
"v_mfma_f32_16x16x32_bf16 v[144:147], v[160:163], v[228:231], v[144:147]\n\t"
"ds_read_b128 v[220:223], v237 offset:57344\n\t"
"v_mfma_f32_16x16x32_bf16 v[148:151], v[164:167], v[228:231], v[148:151]\n\t"
"v_mfma_f32_16x16x32_bf16 v[132:135], v[164:167], v[224:227], v[132:135]\n\t"
"v_mfma_f32_16x16x32_bf16 v[136:139], v[168:171], v[224:227], v[136:139]\n\t"
"v_mfma_f32_16x16x32_bf16 v[152:155], v[168:171], v[228:231], v[152:155]\n\t"
"v_mfma_f32_16x16x32_bf16 v[156:159], v[172:175], v[228:231], v[156:159]\n\t"
"v_mfma_f32_16x16x32_bf16 v[140:143], v[172:175], v[224:227], v[140:143]\n\t"
"ds_read_b128 v[224:227], v237 offset:54272\n\t"
"v_mfma_f32_16x16x32_bf16 v[0:3], v[192:195], v[176:179], v[0:3]\n\t"
"ds_read_b128 v[228:231], v237 offset:58368\n\t"
"v_mfma_f32_16x16x32_bf16 v[16:19], v[196:199], v[176:179], v[16:19]\n\t"
"v_mfma_f32_16x16x32_bf16 v[20:23], v[196:199], v[180:183], v[20:23]\n\t"
"v_mfma_f32_16x16x32_bf16 v[4:7], v[192:195], v[180:183], v[4:7]\n\t"
"v_mfma_f32_16x16x32_bf16 v[8:11], v[192:195], v[184:187], v[8:11]\n\t"
"v_mfma_f32_16x16x32_bf16 v[24:27], v[196:199], v[184:187], v[24:27]\n\t"
"v_mfma_f32_16x16x32_bf16 v[28:31], v[196:199], v[188:191], v[28:31]\n\t"
"v_mfma_f32_16x16x32_bf16 v[12:15], v[192:195], v[188:191], v[12:15]\n\t"
"s_waitcnt lgkmcnt(0)\n\t"
"s_barrier\n\t"
"v_mfma_f32_16x16x32_bf16 v[44:47], v[200:203], v[188:191], v[44:47]\n\t"
"v_add_u32 v232, 0xffff3000, v232\n\t"
"v_mfma_f32_16x16x32_bf16 v[60:63], v[204:207], v[188:191], v[60:63]\n\t"
"v_add_u32 v234, 0xffff3000, v234\n\t"
"v_mfma_f32_16x16x32_bf16 v[56:59], v[204:207], v[184:187], v[56:59]\n\t"
"v_add_u32 v235, 0xffff3000, v235\n\t"
"v_mfma_f32_16x16x32_bf16 v[40:43], v[200:203], v[184:187], v[40:43]\n\t"
"ds_read_b128 v[160:163], v232 offset:0\n\t"
"v_mfma_f32_16x16x32_bf16 v[36:39], v[200:203], v[180:183], v[36:39]\n\t"
"ds_read_b128 v[164:167], v232 offset:2048\n\t"
"v_mfma_f32_16x16x32_bf16 v[52:55], v[204:207], v[180:183], v[52:55]\n\t"
"ds_read_b128 v[168:171], v232 offset:4096\n\t"
"v_mfma_f32_16x16x32_bf16 v[48:51], v[204:207], v[176:179], v[48:51]\n\t"
"ds_read_b128 v[172:175], v232 offset:6144\n\t"
"v_mfma_f32_16x16x32_bf16 v[32:35], v[200:203], v[176:179], v[32:35]\n\t"
"v_add_u32 v236, 0xffff3000, v236\n\t"
"v_mfma_f32_16x16x32_bf16 v[64:67], v[176:179], v[208:211], v[64:67]\n\t"
"ds_read_b64_tr_b16 v[192:193], v234 offset:0\n\t"
"v_mfma_f32_16x16x32_bf16 v[80:83], v[176:179], v[212:215], v[80:83]\n\t"
"ds_read_b64_tr_b16 v[194:195], v234 offset:256\n\t"
"v_mfma_f32_16x16x32_bf16 v[84:87], v[180:183], v[212:215], v[84:87]\n\t"
"v_add_u32 v233, 0xffff3000, v233\n\t"
"v_mfma_f32_16x16x32_bf16 v[68:71], v[180:183], v[208:211], v[68:71]\n\t"
"ds_read_b64_tr_b16 v[196:197], v235 offset:0\n\t"
"v_mfma_f32_16x16x32_bf16 v[72:75], v[184:187], v[208:211], v[72:75]\n\t"
"ds_read_b64_tr_b16 v[198:199], v235 offset:256\n\t"
"v_mfma_f32_16x16x32_bf16 v[88:91], v[184:187], v[212:215], v[88:91]\n\t"
"v_add_u32 v237, 0xffff3000, v237\n\t"
"v_mfma_f32_16x16x32_bf16 v[92:95], v[188:191], v[212:215], v[92:95]\n\t"
"ds_read_b64_tr_b16 v[200:201], v234 offset:1024\n\t"
"v_mfma_f32_16x16x32_bf16 v[76:79], v[188:191], v[208:211], v[76:79]\n\t"
"ds_read_b64_tr_b16 v[202:203], v234 offset:1280\n\t"
"v_mfma_f32_16x16x32_bf16 v[108:111], v[188:191], v[216:219], v[108:111]\n\t"
"ds_read_b64_tr_b16 v[204:205], v235 offset:1024\n\t"
"v_mfma_f32_16x16x32_bf16 v[124:127], v[188:191], v[220:223], v[124:127]\n\t"
"ds_read_b64_tr_b16 v[206:207], v235 offset:1280\n\t"
"v_mfma_f32_16x16x32_bf16 v[120:123], v[184:187], v[220:223], v[120:123]\n\t"
"ds_read_b64_tr_b16 v[208:209], v234 offset:2048\n\t"
"v_mfma_f32_16x16x32_bf16 v[104:107], v[184:187], v[216:219], v[104:107]\n\t"
"ds_read_b64_tr_b16 v[210:211], v234 offset:2304\n\t"
"v_mfma_f32_16x16x32_bf16 v[100:103], v[180:183], v[216:219], v[100:103]\n\t"
"ds_read_b64_tr_b16 v[212:213], v235 offset:2048\n\t"
"v_mfma_f32_16x16x32_bf16 v[116:119], v[180:183], v[220:223], v[116:119]\n\t"
"ds_read_b64_tr_b16 v[214:215], v235 offset:2304\n\t"
"v_mfma_f32_16x16x32_bf16 v[112:115], v[176:179], v[220:223], v[112:115]\n\t"
"v_mfma_f32_16x16x32_bf16 v[96:99], v[176:179], v[216:219], v[96:99]\n\t"
"v_mfma_f32_16x16x32_bf16 v[128:131], v[176:179], v[224:227], v[128:131]\n\t"
"ds_read_b128 v[216:219], v236 offset:0\n\t"
"v_mfma_f32_16x16x32_bf16 v[144:147], v[176:179], v[228:231], v[144:147]\n\t"
"ds_read_b128 v[220:223], v236 offset:4096\n\t"
"v_mfma_f32_16x16x32_bf16 v[148:151], v[180:183], v[228:231], v[148:151]\n\t"
"v_mfma_f32_16x16x32_bf16 v[132:135], v[180:183], v[224:227], v[132:135]\n\t"
"v_mfma_f32_16x16x32_bf16 v[136:139], v[184:187], v[224:227], v[136:139]\n\t"
"s_sub_u32 s56, s56, 1\n\t"
"v_mfma_f32_16x16x32_bf16 v[152:155], v[184:187], v[228:231], v[152:155]\n\t"
"s_cmp_lg_u32 s56, 0\n\t"
"v_mfma_f32_16x16x32_bf16 v[156:159], v[188:191], v[228:231], v[156:159]\n\t"
"v_mfma_f32_16x16x32_bf16 v[140:143], v[188:191], v[224:227], v[140:143]\n\t"
"ds_read_b128 v[224:227], v236 offset:1024\n\t"
"s_cbranch_scc1 L_g1c_%=\n\t"
"s_waitcnt lgkmcnt(3)\n\t"
"v_mfma_f32_16x16x32_bf16 v[0:3], v[192:195], v[160:163], v[0:3]\n\t"
"ds_read_b128 v[228:231], v236 offset:5120\n\t"
"v_mfma_f32_16x16x32_bf16 v[16:19], v[196:199], v[160:163], v[16:19]\n\t"
"ds_read_b128 v[176:179], v233 offset:0\n\t"
"v_mfma_f32_16x16x32_bf16 v[20:23], v[196:199], v[164:167], v[20:23]\n\t"
"ds_read_b128 v[180:183], v233 offset:2048\n\t"
"v_mfma_f32_16x16x32_bf16 v[4:7], v[192:195], v[164:167], v[4:7]\n\t"
"ds_read_b128 v[184:187], v233 offset:4096\n\t"
"v_mfma_f32_16x16x32_bf16 v[8:11], v[192:195], v[168:171], v[8:11]\n\t"
"ds_read_b128 v[188:191], v233 offset:6144\n\t"
"v_mfma_f32_16x16x32_bf16 v[24:27], v[196:199], v[168:171], v[24:27]\n\t"
"v_mfma_f32_16x16x32_bf16 v[28:31], v[196:199], v[172:175], v[28:31]\n\t"
"v_mfma_f32_16x16x32_bf16 v[12:15], v[192:195], v[172:175], v[12:15]\n\t"
"v_mfma_f32_16x16x32_bf16 v[44:47], v[200:203], v[172:175], v[44:47]\n\t"
"ds_read_b64_tr_b16 v[192:193], v234 offset:6144\n\t"
"v_mfma_f32_16x16x32_bf16 v[60:63], v[204:207], v[172:175], v[60:63]\n\t"
"ds_read_b64_tr_b16 v[194:195], v234 offset:6400\n\t"
"v_mfma_f32_16x16x32_bf16 v[56:59], v[204:207], v[168:171], v[56:59]\n\t"
"ds_read_b64_tr_b16 v[196:197], v235 offset:6144\n\t"
"v_mfma_f32_16x16x32_bf16 v[40:43], v[200:203], v[168:171], v[40:43]\n\t"
"ds_read_b64_tr_b16 v[198:199], v235 offset:6400\n\t"
"v_mfma_f32_16x16x32_bf16 v[36:39], v[200:203], v[164:167], v[36:39]\n\t"
"v_mfma_f32_16x16x32_bf16 v[52:55], v[204:207], v[164:167], v[52:55]\n\t"
"v_mfma_f32_16x16x32_bf16 v[48:51], v[204:207], v[160:163], v[48:51]\n\t"
"v_mfma_f32_16x16x32_bf16 v[32:35], v[200:203], v[160:163], v[32:35]\n\t"
"v_mfma_f32_16x16x32_bf16 v[64:67], v[160:163], v[208:211], v[64:67]\n\t"
"ds_read_b64_tr_b16 v[200:201], v234 offset:7168\n\t"
"v_mfma_f32_16x16x32_bf16 v[80:83], v[160:163], v[212:215], v[80:83]\n\t"
"ds_read_b64_tr_b16 v[202:203], v234 offset:7424\n\t"
"v_mfma_f32_16x16x32_bf16 v[84:87], v[164:167], v[212:215], v[84:87]\n\t"
"ds_read_b64_tr_b16 v[204:205], v235 offset:7168\n\t"
"v_mfma_f32_16x16x32_bf16 v[68:71], v[164:167], v[208:211], v[68:71]\n\t"
"ds_read_b64_tr_b16 v[206:207], v235 offset:7424\n\t"
"v_mfma_f32_16x16x32_bf16 v[72:75], v[168:171], v[208:211], v[72:75]\n\t"
"v_mfma_f32_16x16x32_bf16 v[88:91], v[168:171], v[212:215], v[88:91]\n\t"
"v_mfma_f32_16x16x32_bf16 v[92:95], v[172:175], v[212:215], v[92:95]\n\t"
"v_mfma_f32_16x16x32_bf16 v[76:79], v[172:175], v[208:211], v[76:79]\n\t"
"s_waitcnt lgkmcnt(4)\n\t"
"v_mfma_f32_16x16x32_bf16 v[108:111], v[172:175], v[216:219], v[108:111]\n\t"
"ds_read_b64_tr_b16 v[208:209], v234 offset:8192\n\t"
"v_mfma_f32_16x16x32_bf16 v[124:127], v[172:175], v[220:223], v[124:127]\n\t"
"ds_read_b64_tr_b16 v[210:211], v234 offset:8448\n\t"
"v_mfma_f32_16x16x32_bf16 v[120:123], v[168:171], v[220:223], v[120:123]\n\t"
"ds_read_b64_tr_b16 v[212:213], v235 offset:8192\n\t"
"v_mfma_f32_16x16x32_bf16 v[104:107], v[168:171], v[216:219], v[104:107]\n\t"
"ds_read_b64_tr_b16 v[214:215], v235 offset:8448\n\t"
"v_mfma_f32_16x16x32_bf16 v[100:103], v[164:167], v[216:219], v[100:103]\n\t"
"v_mfma_f32_16x16x32_bf16 v[116:119], v[164:167], v[220:223], v[116:119]\n\t"
"v_mfma_f32_16x16x32_bf16 v[112:115], v[160:163], v[220:223], v[112:115]\n\t"
"v_mfma_f32_16x16x32_bf16 v[96:99], v[160:163], v[216:219], v[96:99]\n\t"
"v_mfma_f32_16x16x32_bf16 v[128:131], v[160:163], v[224:227], v[128:131]\n\t"
"ds_read_b128 v[216:219], v237 offset:0\n\t"
"v_mfma_f32_16x16x32_bf16 v[144:147], v[160:163], v[228:231], v[144:147]\n\t"
"ds_read_b128 v[220:223], v237 offset:4096\n\t"
"v_mfma_f32_16x16x32_bf16 v[148:151], v[164:167], v[228:231], v[148:151]\n\t"
"v_mfma_f32_16x16x32_bf16 v[132:135], v[164:167], v[224:227], v[132:135]\n\t"
"v_mfma_f32_16x16x32_bf16 v[136:139], v[168:171], v[224:227], v[136:139]\n\t"
"v_mfma_f32_16x16x32_bf16 v[152:155], v[168:171], v[228:231], v[152:155]\n\t"
"v_mfma_f32_16x16x32_bf16 v[156:159], v[172:175], v[228:231], v[156:159]\n\t"
"v_mfma_f32_16x16x32_bf16 v[140:143], v[172:175], v[224:227], v[140:143]\n\t"
"ds_read_b128 v[224:227], v237 offset:1024\n\t"
"s_waitcnt lgkmcnt(3)\n\t"
"v_mfma_f32_16x16x32_bf16 v[44:47], v[200:203], v[188:191], v[44:47]\n\t"
"ds_read_b128 v[228:231], v237 offset:5120\n\t"
"v_mfma_f32_16x16x32_bf16 v[60:63], v[204:207], v[188:191], v[60:63]\n\t"
"v_mfma_f32_16x16x32_bf16 v[56:59], v[204:207], v[184:187], v[56:59]\n\t"
"v_mfma_f32_16x16x32_bf16 v[40:43], v[200:203], v[184:187], v[40:43]\n\t"
"v_mfma_f32_16x16x32_bf16 v[36:39], v[200:203], v[180:183], v[36:39]\n\t"
"v_mfma_f32_16x16x32_bf16 v[52:55], v[204:207], v[180:183], v[52:55]\n\t"
"v_mfma_f32_16x16x32_bf16 v[48:51], v[204:207], v[176:179], v[48:51]\n\t"
"v_mfma_f32_16x16x32_bf16 v[32:35], v[200:203], v[176:179], v[32:35]\n\t"
"s_waitcnt lgkmcnt(0)\n\t"
"s_barrier\n\t"
"v_mfma_f32_16x16x32_bf16 v[64:67], v[176:179], v[208:211], v[64:67]\n\t"
"v_mfma_f32_16x16x32_bf16 v[80:83], v[176:179], v[212:215], v[80:83]\n\t"
"v_cvt_pk_bf16_f32 v172, v44, v45\n\t"
"v_cvt_pk_bf16_f32 v173, v46, v47\n\t"
"v_mfma_f32_16x16x32_bf16 v[84:87], v[180:183], v[212:215], v[84:87]\n\t"
"v_cvt_pk_bf16_f32 v174, v60, v61\n\t"
"v_cvt_pk_bf16_f32 v175, v62, v63\n\t"
"v_mfma_f32_16x16x32_bf16 v[68:71], v[180:183], v[208:211], v[68:71]\n\t"
"buffer_store_dwordx4 v[172:175], v240, s[24:27], s69 offen offset:128 sc1\n\t"
"ds_write_b128 v240, v[172:175] offset:4224\n\t"
"v_mfma_f32_16x16x32_bf16 v[72:75], v[184:187], v[208:211], v[72:75]\n\t"
"v_cvt_pk_bf16_f32 v168, v40, v41\n\t"
"v_cvt_pk_bf16_f32 v169, v42, v43\n\t"
"v_mfma_f32_16x16x32_bf16 v[88:91], v[184:187], v[212:215], v[88:91]\n\t"
"v_cvt_pk_bf16_f32 v170, v56, v57\n\t"
"v_cvt_pk_bf16_f32 v171, v58, v59\n\t"
"v_mfma_f32_16x16x32_bf16 v[92:95], v[188:191], v[212:215], v[92:95]\n\t"
"buffer_store_dwordx4 v[168:171], v240, s[24:27], s69 offen sc1\n\t"
"ds_write_b128 v240, v[168:171] offset:4096\n\t"
"v_mfma_f32_16x16x32_bf16 v[76:79], v[188:191], v[208:211], v[76:79]\n\t"
"v_cvt_pk_bf16_f32 v164, v36, v37\n\t"
"v_cvt_pk_bf16_f32 v165, v38, v39\n\t"
"v_mfma_f32_16x16x32_bf16 v[0:3], v[192:195], v[176:179], v[0:3]\n\t"
"v_cvt_pk_bf16_f32 v166, v52, v53\n\t"
"v_cvt_pk_bf16_f32 v167, v54, v55\n\t"
"v_mfma_f32_16x16x32_bf16 v[16:19], v[196:199], v[176:179], v[16:19]\n\t"
"buffer_store_dwordx4 v[164:167], v240, s[24:27], s68 offen offset:128 sc1\n\t"
"ds_write_b128 v240, v[164:167] offset:128\n\t"
"v_mfma_f32_16x16x32_bf16 v[20:23], v[196:199], v[180:183], v[20:23]\n\t"
"v_cvt_pk_bf16_f32 v160, v32, v33\n\t"
"v_cvt_pk_bf16_f32 v161, v34, v35\n\t"
"v_mfma_f32_16x16x32_bf16 v[4:7], v[192:195], v[180:183], v[4:7]\n\t"
"v_cvt_pk_bf16_f32 v162, v48, v49\n\t"
"v_cvt_pk_bf16_f32 v163, v50, v51\n\t"
"v_mfma_f32_16x16x32_bf16 v[8:11], v[192:195], v[184:187], v[8:11]\n\t"
"buffer_store_dwordx4 v[160:163], v240, s[24:27], s68 offen sc1\n\t"
"ds_write_b128 v240, v[160:163] offset:0\n\t"
"v_mfma_f32_16x16x32_bf16 v[24:27], v[196:199], v[184:187], v[24:27]\n\t"
"v_cvt_pk_bf16_f32 v200, v64, v65\n\t"
"v_cvt_pk_bf16_f32 v201, v66, v67\n\t"
"v_mfma_f32_16x16x32_bf16 v[28:31], v[196:199], v[188:191], v[28:31]\n\t"
"v_cvt_pk_bf16_f32 v202, v68, v69\n\t"
"v_cvt_pk_bf16_f32 v203, v70, v71\n\t"
"v_mfma_f32_16x16x32_bf16 v[12:15], v[192:195], v[188:191], v[12:15]\n\t"
"buffer_store_dwordx4 v[200:203], v241, s[24:27], s68 offen sc1\n\t"
"ds_write_b128 v241, v[200:203] offset:0\n\t"
"v_mfma_f32_16x16x32_bf16 v[108:111], v[188:191], v[216:219], v[108:111]\n\t"
"v_cvt_pk_bf16_f32 v204, v72, v73\n\t"
"v_cvt_pk_bf16_f32 v205, v74, v75\n\t"
"v_mfma_f32_16x16x32_bf16 v[124:127], v[188:191], v[220:223], v[124:127]\n\t"
"v_cvt_pk_bf16_f32 v206, v76, v77\n\t"
"v_cvt_pk_bf16_f32 v207, v78, v79\n\t"
"v_mfma_f32_16x16x32_bf16 v[120:123], v[184:187], v[220:223], v[120:123]\n\t"
"buffer_store_dwordx4 v[204:207], v241, s[24:27], s69 offen sc1\n\t"
"ds_write_b128 v241, v[204:207] offset:4096\n\t"
"v_mfma_f32_16x16x32_bf16 v[104:107], v[184:187], v[216:219], v[104:107]\n\t"
"v_cvt_pk_bf16_f32 v208, v80, v81\n\t"
"v_cvt_pk_bf16_f32 v209, v82, v83\n\t"
"v_mfma_f32_16x16x32_bf16 v[100:103], v[180:183], v[216:219], v[100:103]\n\t"
"v_cvt_pk_bf16_f32 v210, v84, v85\n\t"
"v_cvt_pk_bf16_f32 v211, v86, v87\n\t"
"v_mfma_f32_16x16x32_bf16 v[116:119], v[180:183], v[220:223], v[116:119]\n\t"
"buffer_store_dwordx4 v[208:211], v241, s[24:27], s68 offen offset:256 sc1\n\t"
"ds_write_b128 v241, v[208:211] offset:256\n\t"
"v_mfma_f32_16x16x32_bf16 v[112:115], v[176:179], v[220:223], v[112:115]\n\t"
"v_cvt_pk_bf16_f32 v212, v88, v89\n\t"
"v_cvt_pk_bf16_f32 v213, v90, v91\n\t"
"v_mfma_f32_16x16x32_bf16 v[96:99], v[176:179], v[216:219], v[96:99]\n\t"
"v_cvt_pk_bf16_f32 v214, v92, v93\n\t"
"v_cvt_pk_bf16_f32 v215, v94, v95\n\t"
"v_mfma_f32_16x16x32_bf16 v[128:131], v[176:179], v[224:227], v[128:131]\n\t"
"buffer_store_dwordx4 v[212:215], v241, s[24:27], s69 offen offset:256 sc1\n\t"
"ds_write_b128 v241, v[212:215] offset:4352\n\t"
"v_mfma_f32_16x16x32_bf16 v[144:147], v[176:179], v[228:231], v[144:147]\n\t"
"v_mul_f32 v12, s63, v12\n\t"
"v_mul_f32 v13, s63, v13\n\t"
"v_mfma_f32_16x16x32_bf16 v[148:151], v[180:183], v[228:231], v[148:151]\n\t"
"v_mul_f32 v14, s63, v14\n\t"
"v_mul_f32 v15, s63, v15\n\t"
"v_mfma_f32_16x16x32_bf16 v[132:135], v[180:183], v[224:227], v[132:135]\n\t"
"v_mul_f32 v28, s63, v28\n\t"
"v_mul_f32 v29, s63, v29\n\t"
"v_mfma_f32_16x16x32_bf16 v[136:139], v[184:187], v[224:227], v[136:139]\n\t"
"v_mul_f32 v30, s63, v30\n\t"
"v_mul_f32 v31, s63, v31\n\t"
"v_mfma_f32_16x16x32_bf16 v[152:155], v[184:187], v[228:231], v[152:155]\n\t"
"v_cvt_pk_bf16_f32 v192, v12, v13\n\t"
"v_cvt_pk_bf16_f32 v193, v14, v15\n\t"
"v_mfma_f32_16x16x32_bf16 v[156:159], v[188:191], v[228:231], v[156:159]\n\t"
"v_cvt_pk_bf16_f32 v194, v28, v29\n\t"
"v_cvt_pk_bf16_f32 v195, v30, v31\n\t"
"v_mfma_f32_16x16x32_bf16 v[140:143], v[188:191], v[224:227], v[140:143]\n\t"
"ds_write_b128 v242, v[192:195] offset:2112\n\t"
"v_mul_f32 v8, s63, v8\n\t"
"v_mul_f32 v9, s63, v9\n\t"
"v_mul_f32 v10, s63, v10\n\t"
"v_mul_f32 v11, s63, v11\n\t"
"v_mul_f32 v24, s63, v24\n\t"
"v_mul_f32 v25, s63, v25\n\t"
"v_mul_f32 v26, s63, v26\n\t"
"v_mul_f32 v27, s63, v27\n\t"
"v_cvt_pk_bf16_f32 v196, v8, v9\n\t"
"v_cvt_pk_bf16_f32 v197, v10, v11\n\t"
"v_cvt_pk_bf16_f32 v198, v24, v25\n\t"
"v_cvt_pk_bf16_f32 v199, v26, v27\n\t"
"ds_write_b128 v242, v[196:199] offset:2048\n\t"
"v_mul_f32 v4, s63, v4\n\t"
"v_mul_f32 v5, s63, v5\n\t"
"v_mul_f32 v6, s63, v6\n\t"
"v_mul_f32 v7, s63, v7\n\t"
"v_mul_f32 v20, s63, v20\n\t"
"v_mul_f32 v21, s63, v21\n\t"
"v_mul_f32 v22, s63, v22\n\t"
"v_mul_f32 v23, s63, v23\n\t"
"v_cvt_pk_bf16_f32 v172, v4, v5\n\t"
"v_cvt_pk_bf16_f32 v173, v6, v7\n\t"
"v_cvt_pk_bf16_f32 v174, v20, v21\n\t"
"v_cvt_pk_bf16_f32 v175, v22, v23\n\t"
"ds_write_b128 v242, v[172:175] offset:64\n\t"
"v_mul_f32 v0, s63, v0\n\t"
"v_mul_f32 v1, s63, v1\n\t"
"v_mul_f32 v2, s63, v2\n\t"
"v_mul_f32 v3, s63, v3\n\t"
"v_mul_f32 v16, s63, v16\n\t"
"v_mul_f32 v17, s63, v17\n\t"
"v_mul_f32 v18, s63, v18\n\t"
"v_mul_f32 v19, s63, v19\n\t"
"v_cvt_pk_bf16_f32 v168, v0, v1\n\t"
"v_cvt_pk_bf16_f32 v169, v2, v3\n\t"
"v_cvt_pk_bf16_f32 v170, v16, v17\n\t"
"v_cvt_pk_bf16_f32 v171, v18, v19\n\t"
"ds_write_b128 v242, v[168:171] offset:0\n\t"
"v_cvt_pk_bf16_f32 v164, v96, v97\n\t"
"v_cvt_pk_bf16_f32 v165, v98, v99\n\t"
"v_cvt_pk_bf16_f32 v166, v100, v101\n\t"
"v_cvt_pk_bf16_f32 v167, v102, v103\n\t"
"ds_write_b128 v244, v[164:167] offset:0\n\t"
"v_cvt_pk_bf16_f32 v160, v104, v105\n\t"
"v_cvt_pk_bf16_f32 v161, v106, v107\n\t"
"v_cvt_pk_bf16_f32 v162, v108, v109\n\t"
"v_cvt_pk_bf16_f32 v163, v110, v111\n\t"
"ds_write_b128 v244, v[160:163] offset:1024\n\t"
"v_cvt_pk_bf16_f32 v216, v112, v113\n\t"
"v_cvt_pk_bf16_f32 v217, v114, v115\n\t"
"v_cvt_pk_bf16_f32 v218, v116, v117\n\t"
"v_cvt_pk_bf16_f32 v219, v118, v119\n\t"
"ds_write_b128 v244, v[216:219] offset:2048\n\t"
"v_cvt_pk_bf16_f32 v220, v120, v121\n\t"
"v_cvt_pk_bf16_f32 v221, v122, v123\n\t"
"v_cvt_pk_bf16_f32 v222, v124, v125\n\t"
"v_cvt_pk_bf16_f32 v223, v126, v127\n\t"
"ds_write_b128 v244, v[220:223] offset:3072\n\t"
"v_cvt_pk_bf16_f32 v176, -v128, -v129\n\t"
"v_cvt_pk_bf16_f32 v177, -v130, -v131\n\t"
"v_cvt_pk_bf16_f32 v178, -v132, -v133\n\t"
"v_cvt_pk_bf16_f32 v179, -v134, -v135\n\t"
"ds_write_b128 v244, v[176:179] offset:4096\n\t"
"v_cvt_pk_bf16_f32 v180, -v136, -v137\n\t"
"v_cvt_pk_bf16_f32 v181, -v138, -v139\n\t"
"v_cvt_pk_bf16_f32 v182, -v140, -v141\n\t"
"v_cvt_pk_bf16_f32 v183, -v142, -v143\n\t"
"ds_write_b128 v244, v[180:183] offset:5120\n\t"
"v_cvt_pk_bf16_f32 v184, -v144, -v145\n\t"
"v_cvt_pk_bf16_f32 v185, -v146, -v147\n\t"
"v_cvt_pk_bf16_f32 v186, -v148, -v149\n\t"
"v_cvt_pk_bf16_f32 v187, -v150, -v151\n\t"
"ds_write_b128 v244, v[184:187] offset:6144\n\t"
"v_cvt_pk_bf16_f32 v188, -v152, -v153\n\t"
"v_cvt_pk_bf16_f32 v189, -v154, -v155\n\t"
"v_cvt_pk_bf16_f32 v190, -v156, -v157\n\t"
"v_cvt_pk_bf16_f32 v191, -v158, -v159\n\t"
"ds_write_b128 v244, v[188:191] offset:7168\n\t"
"s_waitcnt lgkmcnt(8)\n\t"
"ds_read_b128 v[96:99], v243 offset:0\n\t"
"ds_read_b128 v[100:103], v243 offset:1024\n\t"
"ds_read_b128 v[104:107], v243 offset:2048\n\t"
"ds_read_b128 v[108:111], v243 offset:3072\n\t"
"s_waitcnt lgkmcnt(0)\n\t"
"s_barrier\n\t"
"v_mov_b32 v156, v238\n\t"
"s_mov_b32 s73, 0\n\t"
"s_mov_b32 s72, 0\n\t"
"ds_read_b128 v[128:131], v156 offset:0\n\t"
"ds_read_b128 v[132:135], v156 offset:1024\n\t"
"v_and_b32 v241, 2, v253\n\t"
"v_cmp_eq_u32 vcc, 0, v241\n\t"
"v_mov_b32 v158, 0x44444444\n\t"
"v_mov_b32 v241, 0xeeeeeeee\n\t"
"v_and_b32 v240, 1, v253\n\t"
"v_cndmask_b32 v158, v241, v158, vcc\n\t"
"v_cmp_eq_u32 vcc, 0, v240\n\t"
"v_mov_b32 v241, 0x3f803f80\n\t"
"s_nop 0\n\t"
"v_cndmask_b32 v152, 0, v241, vcc\n\t"
"v_mov_b32 v153, v152\n\t"
"v_xor_b32 v154, v241, v152\n\t"
"v_xor_b32 v155, v241, v152\n\t"
"s_waitcnt lgkmcnt(0)\n\t"
"v_mfma_f32_32x32x16_bf16 v[32:47], v[128:131], v[96:99], 0\n\t"
"v_mfma_f32_32x32x16_bf16 v[48:63], v[128:131], v[104:107], 0\n\t"
"v_mfma_f32_32x32x16_bf16 v[32:47], v[132:135], v[100:103], v[32:47]\n\t"
"v_mfma_f32_32x32x16_bf16 v[48:63], v[132:135], v[108:111], v[48:63]\n\t"
"v_mov_b32 v192, 0\n\t"
"v_mov_b32 v193, 0\n\t"
"v_mov_b32 v194, 0\n\t"
"v_mov_b32 v195, 0\n\t"
"v_mov_b32 v196, 0\n\t"
"v_mov_b32 v197, 0\n\t"
"v_mov_b32 v198, 0\n\t"
"v_mov_b32 v199, 0\n\t"
"v_mov_b32 v200, 0\n\t"
"v_mov_b32 v201, 0\n\t"
"v_mov_b32 v202, 0\n\t"
"v_mov_b32 v203, 0\n\t"
"v_mov_b32 v204, 0\n\t"
"v_mov_b32 v205, 0\n\t"
"v_mov_b32 v206, 0\n\t"
"v_mov_b32 v207, 0\n\t"
"v_mov_b32 v208, 0\n\t"
"v_mov_b32 v209, 0\n\t"
"v_mov_b32 v210, 0\n\t"
"v_mov_b32 v211, 0\n\t"
"v_mov_b32 v212, 0\n\t"
"v_mov_b32 v213, 0\n\t"
"v_mov_b32 v214, 0\n\t"
"v_mov_b32 v215, 0\n\t"
"v_mov_b32 v216, 0\n\t"
"v_mov_b32 v217, 0\n\t"
"v_mov_b32 v218, 0\n\t"
"v_mov_b32 v219, 0\n\t"
"v_mov_b32 v220, 0\n\t"
"v_mov_b32 v221, 0\n\t"
"v_mov_b32 v222, 0\n\t"
"v_mov_b32 v223, 0\n\t"
"v_mov_b32 v224, 0\n\t"
"v_mov_b32 v225, 0\n\t"
"v_mov_b32 v226, 0\n\t"
"v_mov_b32 v227, 0\n\t"
"v_mov_b32 v228, 0\n\t"
"v_mov_b32 v229, 0\n\t"
"v_mov_b32 v230, 0\n\t"
"v_mov_b32 v231, 0\n\t"
"v_mov_b32 v120, 0\n\t"
"v_mov_b32 v121, 0\n\t"
"v_mov_b32 v122, 0\n\t"
"v_mov_b32 v123, 0\n\t"
"v_mov_b32 v124, 0\n\t"
"v_mov_b32 v125, 0\n\t"
"v_mov_b32 v126, 0\n\t"
"v_mov_b32 v127, 0\n\t"
"v_mov_b32 v136, 0\n\t"
"v_mov_b32 v137, 0\n\t"
"v_mov_b32 v138, 0\n\t"
"v_mov_b32 v139, 0\n\t"
"v_mov_b32 v140, 0\n\t"
"v_mov_b32 v141, 0\n\t"
"v_mov_b32 v142, 0\n\t"
"v_mov_b32 v143, 0\n\t"
"v_max3_f32 v240, v32, v33, v34\n\t"
"v_max3_f32 v241, v35, v36, v37\n\t"
"v_max3_f32 v240, v240, v38, v39\n\t"
"v_max3_f32 v241, v241, v40, v41\n\t"
"v_max3_f32 v240, v240, v42, v43\n\t"
"v_max3_f32 v241, v241, v44, v45\n\t"
"v_max3_f32 v240, v240, v46, v47\n\t"
"v_max_f32 v240, v240, v241\n\t"
"v_mov_b32 v241, v240\n\t"
"s_nop 1\n\t"
"v_permlane32_swap_b32 v240, v241\n\t"
"s_nop 1\n\t"
"v_max_f32 v240, v240, v241\n\t"
"v_sub_f32 v64, 0xc1800000, v240\n\t"
"v_mov_b32 v65, v64\n\t"
"v_mov_b32 v66, v64\n\t"
"v_mov_b32 v67, v64\n\t"
"v_mov_b32 v68, v64\n\t"
"v_mov_b32 v69, v64\n\t"
"v_mov_b32 v70, v64\n\t"
"v_mov_b32 v71, v64\n\t"
"v_mov_b32 v72, v64\n\t"
"v_mov_b32 v73, v64\n\t"
"v_mov_b32 v74, v64\n\t"
"v_mov_b32 v75, v64\n\t"
"v_mov_b32 v76, v64\n\t"
"v_mov_b32 v77, v64\n\t"
"v_mov_b32 v78, v64\n\t"
"v_mov_b32 v79, v64\n\t"
"s_nop 1\n\t"
"v_mfma_f32_32x32x16_bf16 v[32:47], v[128:131], v[96:99], v[64:79]\n\t"
"v_mfma_f32_32x32x16_bf16 v[32:47], v[132:135], v[100:103], v[32:47]\n\t"
"v_max3_f32 v240, v48, v49, v50\n\t"
"v_max3_f32 v241, v51, v52, v53\n\t"
"v_max3_f32 v240, v240, v54, v55\n\t"
"v_max3_f32 v241, v241, v56, v57\n\t"
"v_max3_f32 v240, v240, v58, v59\n\t"
"v_max3_f32 v241, v241, v60, v61\n\t"
"v_max3_f32 v240, v240, v62, v63\n\t"
"v_max_f32 v240, v240, v241\n\t"
"v_mov_b32 v241, v240\n\t"
"s_nop 1\n\t"
"v_permlane32_swap_b32 v240, v241\n\t"
"s_nop 1\n\t"
"v_max_f32 v240, v240, v241\n\t"
"v_sub_f32 v80, 0xc1800000, v240\n\t"
"v_mov_b32 v81, v80\n\t"
"v_mov_b32 v82, v80\n\t"
"v_mov_b32 v83, v80\n\t"
"v_mov_b32 v84, v80\n\t"
"v_mov_b32 v85, v80\n\t"
"v_mov_b32 v86, v80\n\t"
"v_mov_b32 v87, v80\n\t"
"v_mov_b32 v88, v80\n\t"
"v_mov_b32 v89, v80\n\t"
"v_mov_b32 v90, v80\n\t"
"v_mov_b32 v91, v80\n\t"
"v_mov_b32 v92, v80\n\t"
"v_mov_b32 v93, v80\n\t"
"v_mov_b32 v94, v80\n\t"
"v_mov_b32 v95, v80\n\t"
"s_nop 1\n\t"
"v_mfma_f32_32x32x16_bf16 v[48:63], v[128:131], v[104:107], v[80:95]\n\t"
".p2align 6\n\t"
"s_nop 0\n\t"
"s_nop 0\n\t"
"L_att_%=:\n\t"
"ds_read_b128 v[128:131], v156 offset:4096\n\t"
"v_exp_f32 v32, v32\n\t"
"v_exp_f32 v33, v33\n\t"
"v_exp_f32 v34, v34\n\t"
"v_mfma_f32_32x32x16_bf16 v[208:223], v[136:139], v[120:123], v[208:223]\n\t"
"ds_read_b128 v[136:139], v156 offset:2048\n\t"
"v_exp_f32 v35, v35\n\t"
"v_exp_f32 v36, v36\n\t"
"v_exp_f32 v37, v37\n\t"
"v_mfma_f32_32x32x16_bf16 v[48:63], v[132:135], v[108:111], v[48:63]\n\t"
"ds_read_b128 v[132:135], v156 offset:5120\n\t"
"v_exp_f32 v38, v38\n\t"
"v_exp_f32 v39, v39\n\t"
"v_cvt_pk_bf16_f32 v112, v32, v33\n\t"
"v_cvt_pk_bf16_f32 v113, v34, v35\n\t"
"v_mfma_f32_32x32x16_bf16 v[208:223], v[140:143], v[124:127], v[208:223]\n\t"
"ds_read_b128 v[140:143], v156 offset:3072\n\t"
"v_exp_f32 v40, v40\n\t"
"v_exp_f32 v41, v41\n\t"
"v_cvt_pk_bf16_f32 v114, v36, v37\n\t"
"v_cvt_pk_bf16_f32 v115, v38, v39\n\t"
"v_smfmac_f32_16x16x64_bf16 v[228:231], v[152:155], v[120:127], v158\n\t"
"s_waitcnt lgkmcnt(1)\n\t"
"v_exp_f32 v42, v42\n\t"
"v_exp_f32 v43, v43\n\t"
"v_exp_f32 v44, v44\n\t"
"v_exp_f32 v45, v45\n\t"
"v_exp_f32 v46, v46\n\t"
"v_exp_f32 v47, v47\n\t"
"v_cvt_pk_bf16_f32 v116, v40, v41\n\t"
"v_cvt_pk_bf16_f32 v117, v42, v43\n\t"
"v_cvt_pk_bf16_f32 v118, v44, v45\n\t"
"v_cvt_pk_bf16_f32 v119, v46, v47\n\t"
"v_mfma_f32_32x32x16_bf16 v[32:47], v[128:131], v[96:99], v[64:79]\n\t"
"s_waitcnt lgkmcnt(0)\n\t"
"v_exp_f32 v48, v48\n\t"
"v_exp_f32 v49, v49\n\t"
"v_exp_f32 v50, v50\n\t"
"v_mfma_f32_32x32x16_bf16 v[192:207], v[136:139], v[112:115], v[192:207]\n\t"
"s_cmp_eq_u32 s72, 0\n\t"
"v_exp_f32 v51, v51\n\t"
"v_exp_f32 v52, v52\n\t"
"v_exp_f32 v53, v53\n\t"
"v_mfma_f32_32x32x16_bf16 v[32:47], v[132:135], v[100:103], v[32:47]\n\t"
"s_cbranch_scc1 L_att_pub_%=\n\t"
"L_att_pub_r_%=:\n\t"
"v_exp_f32 v54, v54\n\t"
"v_exp_f32 v55, v55\n\t"
"v_cvt_pk_bf16_f32 v120, v48, v49\n\t"
"v_cvt_pk_bf16_f32 v121, v50, v51\n\t"
"v_mfma_f32_32x32x16_bf16 v[192:207], v[140:143], v[116:119], v[192:207]\n\t"
"v_exp_f32 v56, v56\n\t"
"v_exp_f32 v57, v57\n\t"
"v_cvt_pk_bf16_f32 v122, v52, v53\n\t"
"v_cvt_pk_bf16_f32 v123, v54, v55\n\t"
"v_smfmac_f32_16x16x64_bf16 v[224:227], v[152:155], v[112:119], v158\n\t"
"v_exp_f32 v58, v58\n\t"
"v_exp_f32 v59, v59\n\t"
"v_exp_f32 v60, v60\n\t"
"v_exp_f32 v61, v61\n\t"
"v_exp_f32 v62, v62\n\t"
"v_exp_f32 v63, v63\n\t"
"v_cvt_pk_bf16_f32 v124, v56, v57\n\t"
"v_cvt_pk_bf16_f32 v125, v58, v59\n\t"
"v_cvt_pk_bf16_f32 v126, v60, v61\n\t"
"v_cvt_pk_bf16_f32 v127, v62, v63\n\t"
"v_mfma_f32_32x32x16_bf16 v[48:63], v[128:131], v[104:107], v[80:95]\n\t"
"ds_read_b128 v[128:131], v156 offset:8192\n\t"
"v_exp_f32 v32, v32\n\t"
"v_exp_f32 v33, v33\n\t"
"v_exp_f32 v34, v34\n\t"
"v_mfma_f32_32x32x16_bf16 v[208:223], v[136:139], v[120:123], v[208:223]\n\t"
"ds_read_b128 v[136:139], v156 offset:6144\n\t"
"v_exp_f32 v35, v35\n\t"
"v_exp_f32 v36, v36\n\t"
"v_exp_f32 v37, v37\n\t"
"v_mfma_f32_32x32x16_bf16 v[48:63], v[132:135], v[108:111], v[48:63]\n\t"
"ds_read_b128 v[132:135], v156 offset:9216\n\t"
"v_exp_f32 v38, v38\n\t"
"v_exp_f32 v39, v39\n\t"
"v_cvt_pk_bf16_f32 v112, v32, v33\n\t"
"v_cvt_pk_bf16_f32 v113, v34, v35\n\t"
"v_mfma_f32_32x32x16_bf16 v[208:223], v[140:143], v[124:127], v[208:223]\n\t"
"ds_read_b128 v[140:143], v156 offset:7168\n\t"
"v_exp_f32 v40, v40\n\t"
"v_exp_f32 v41, v41\n\t"
"v_cvt_pk_bf16_f32 v114, v36, v37\n\t"
"v_cvt_pk_bf16_f32 v115, v38, v39\n\t"
"v_smfmac_f32_16x16x64_bf16 v[228:231], v[152:155], v[120:127], v158\n\t"
"s_waitcnt lgkmcnt(1)\n\t"
"v_exp_f32 v42, v42\n\t"
"v_exp_f32 v43, v43\n\t"
"v_exp_f32 v44, v44\n\t"
"v_exp_f32 v45, v45\n\t"
"v_exp_f32 v46, v46\n\t"
"v_exp_f32 v47, v47\n\t"
"v_cvt_pk_bf16_f32 v116, v40, v41\n\t"
"v_cvt_pk_bf16_f32 v117, v42, v43\n\t"
"v_cvt_pk_bf16_f32 v118, v44, v45\n\t"
"v_cvt_pk_bf16_f32 v119, v46, v47\n\t"
"v_mfma_f32_32x32x16_bf16 v[32:47], v[128:131], v[96:99], v[64:79]\n\t"
"s_waitcnt lgkmcnt(0)\n\t"
"v_exp_f32 v48, v48\n\t"
"v_exp_f32 v49, v49\n\t"
"v_exp_f32 v50, v50\n\t"
"v_mfma_f32_32x32x16_bf16 v[192:207], v[136:139], v[112:115], v[192:207]\n\t"
"v_exp_f32 v51, v51\n\t"
"v_exp_f32 v52, v52\n\t"
"v_exp_f32 v53, v53\n\t"
"v_mfma_f32_32x32x16_bf16 v[32:47], v[132:135], v[100:103], v[32:47]\n\t"
"v_exp_f32 v54, v54\n\t"
"v_exp_f32 v55, v55\n\t"
"v_cvt_pk_bf16_f32 v120, v48, v49\n\t"
"v_cvt_pk_bf16_f32 v121, v50, v51\n\t"
"v_mfma_f32_32x32x16_bf16 v[192:207], v[140:143], v[116:119], v[192:207]\n\t"
"v_exp_f32 v56, v56\n\t"
"v_exp_f32 v57, v57\n\t"
"v_cvt_pk_bf16_f32 v122, v52, v53\n\t"
"v_cvt_pk_bf16_f32 v123, v54, v55\n\t"
"v_smfmac_f32_16x16x64_bf16 v[224:227], v[152:155], v[112:119], v158\n\t"
"v_exp_f32 v58, v58\n\t"
"v_exp_f32 v59, v59\n\t"
"v_exp_f32 v60, v60\n\t"
"v_exp_f32 v61, v61\n\t"
"v_exp_f32 v62, v62\n\t"
"v_exp_f32 v63, v63\n\t"
"v_cvt_pk_bf16_f32 v124, v56, v57\n\t"
"v_cvt_pk_bf16_f32 v125, v58, v59\n\t"
"v_cvt_pk_bf16_f32 v126, v60, v61\n\t"
"v_cvt_pk_bf16_f32 v127, v62, v63\n\t"
"v_mfma_f32_32x32x16_bf16 v[48:63], v[128:131], v[104:107], v[80:95]\n\t"
"ds_read_b128 v[128:131], v156 offset:12288\n\t"
"v_exp_f32 v32, v32\n\t"
"v_exp_f32 v33, v33\n\t"
"v_exp_f32 v34, v34\n\t"
"v_mfma_f32_32x32x16_bf16 v[208:223], v[136:139], v[120:123], v[208:223]\n\t"
"ds_read_b128 v[136:139], v156 offset:10240\n\t"
"v_exp_f32 v35, v35\n\t"
"v_exp_f32 v36, v36\n\t"
"v_exp_f32 v37, v37\n\t"
"v_mfma_f32_32x32x16_bf16 v[48:63], v[132:135], v[108:111], v[48:63]\n\t"
"ds_read_b128 v[132:135], v156 offset:13312\n\t"
"v_exp_f32 v38, v38\n\t"
"v_exp_f32 v39, v39\n\t"
"v_cvt_pk_bf16_f32 v112, v32, v33\n\t"
"v_cvt_pk_bf16_f32 v113, v34, v35\n\t"
"v_mfma_f32_32x32x16_bf16 v[208:223], v[140:143], v[124:127], v[208:223]\n\t"
"ds_read_b128 v[140:143], v156 offset:11264\n\t"
"v_exp_f32 v40, v40\n\t"
"v_exp_f32 v41, v41\n\t"
"v_cvt_pk_bf16_f32 v114, v36, v37\n\t"
"v_cvt_pk_bf16_f32 v115, v38, v39\n\t"
"v_smfmac_f32_16x16x64_bf16 v[228:231], v[152:155], v[120:127], v158\n\t"
"s_waitcnt lgkmcnt(1)\n\t"
"v_exp_f32 v42, v42\n\t"
"v_exp_f32 v43, v43\n\t"
"v_exp_f32 v44, v44\n\t"
"v_exp_f32 v45, v45\n\t"
"v_exp_f32 v46, v46\n\t"
"v_exp_f32 v47, v47\n\t"
"v_cvt_pk_bf16_f32 v116, v40, v41\n\t"
"v_cvt_pk_bf16_f32 v117, v42, v43\n\t"
"v_cvt_pk_bf16_f32 v118, v44, v45\n\t"
"v_cvt_pk_bf16_f32 v119, v46, v47\n\t"
"v_mfma_f32_32x32x16_bf16 v[32:47], v[128:131], v[96:99], v[64:79]\n\t"
"s_waitcnt lgkmcnt(0)\n\t"
"v_exp_f32 v48, v48\n\t"
"v_exp_f32 v49, v49\n\t"
"v_exp_f32 v50, v50\n\t"
"v_mfma_f32_32x32x16_bf16 v[192:207], v[136:139], v[112:115], v[192:207]\n\t"
"v_exp_f32 v51, v51\n\t"
"v_exp_f32 v52, v52\n\t"
"v_exp_f32 v53, v53\n\t"
"v_mfma_f32_32x32x16_bf16 v[32:47], v[132:135], v[100:103], v[32:47]\n\t"
"v_exp_f32 v54, v54\n\t"
"v_exp_f32 v55, v55\n\t"
"v_cvt_pk_bf16_f32 v120, v48, v49\n\t"
"v_cvt_pk_bf16_f32 v121, v50, v51\n\t"
"v_mfma_f32_32x32x16_bf16 v[192:207], v[140:143], v[116:119], v[192:207]\n\t"
"v_exp_f32 v56, v56\n\t"
"v_exp_f32 v57, v57\n\t"
"v_cvt_pk_bf16_f32 v122, v52, v53\n\t"
"v_cvt_pk_bf16_f32 v123, v54, v55\n\t"
"v_smfmac_f32_16x16x64_bf16 v[224:227], v[152:155], v[112:119], v158\n\t"
"v_exp_f32 v58, v58\n\t"
"v_exp_f32 v59, v59\n\t"
"v_exp_f32 v60, v60\n\t"
"v_exp_f32 v61, v61\n\t"
"v_exp_f32 v62, v62\n\t"
"v_exp_f32 v63, v63\n\t"
"v_cvt_pk_bf16_f32 v124, v56, v57\n\t"
"v_cvt_pk_bf16_f32 v125, v58, v59\n\t"
"v_cvt_pk_bf16_f32 v126, v60, v61\n\t"
"v_cvt_pk_bf16_f32 v127, v62, v63\n\t"
"v_mfma_f32_32x32x16_bf16 v[48:63], v[128:131], v[104:107], v[80:95]\n\t"
"ds_read_b128 v[128:131], v156 offset:16384\n\t"
"v_exp_f32 v32, v32\n\t"
"v_exp_f32 v33, v33\n\t"
"v_exp_f32 v34, v34\n\t"
"v_mfma_f32_32x32x16_bf16 v[208:223], v[136:139], v[120:123], v[208:223]\n\t"
"ds_read_b128 v[136:139], v156 offset:14336\n\t"
"v_exp_f32 v35, v35\n\t"
"v_exp_f32 v36, v36\n\t"
"v_exp_f32 v37, v37\n\t"
"v_mfma_f32_32x32x16_bf16 v[48:63], v[132:135], v[108:111], v[48:63]\n\t"
"ds_read_b128 v[132:135], v156 offset:17408\n\t"
"v_exp_f32 v38, v38\n\t"
"v_exp_f32 v39, v39\n\t"
"v_cvt_pk_bf16_f32 v112, v32, v33\n\t"
"v_cvt_pk_bf16_f32 v113, v34, v35\n\t"
"v_mfma_f32_32x32x16_bf16 v[208:223], v[140:143], v[124:127], v[208:223]\n\t"
"ds_read_b128 v[140:143], v156 offset:15360\n\t"
"v_exp_f32 v40, v40\n\t"
"v_exp_f32 v41, v41\n\t"
"v_cvt_pk_bf16_f32 v114, v36, v37\n\t"
"v_cvt_pk_bf16_f32 v115, v38, v39\n\t"
"v_smfmac_f32_16x16x64_bf16 v[228:231], v[152:155], v[120:127], v158\n\t"
"s_waitcnt lgkmcnt(1)\n\t"
"v_exp_f32 v42, v42\n\t"
"v_exp_f32 v43, v43\n\t"
"v_exp_f32 v44, v44\n\t"
"v_exp_f32 v45, v45\n\t"
"v_exp_f32 v46, v46\n\t"
"v_exp_f32 v47, v47\n\t"
"v_cvt_pk_bf16_f32 v116, v40, v41\n\t"
"v_cvt_pk_bf16_f32 v117, v42, v43\n\t"
"v_cvt_pk_bf16_f32 v118, v44, v45\n\t"
"v_cvt_pk_bf16_f32 v119, v46, v47\n\t"
"v_mfma_f32_32x32x16_bf16 v[32:47], v[128:131], v[96:99], v[64:79]\n\t"
"s_waitcnt lgkmcnt(0)\n\t"
"v_exp_f32 v48, v48\n\t"
"v_exp_f32 v49, v49\n\t"
"v_exp_f32 v50, v50\n\t"
"v_mfma_f32_32x32x16_bf16 v[192:207], v[136:139], v[112:115], v[192:207]\n\t"
"s_cmp_eq_u32 s72, 7\n\t"
"v_exp_f32 v51, v51\n\t"
"v_exp_f32 v52, v52\n\t"
"v_exp_f32 v53, v53\n\t"
"v_mfma_f32_32x32x16_bf16 v[32:47], v[132:135], v[100:103], v[32:47]\n\t"
"s_cbranch_scc1 L_att_g_%=\n\t"
"L_att_g_r_%=:\n\t"
"v_exp_f32 v54, v54\n\t"
"v_exp_f32 v55, v55\n\t"
"v_cvt_pk_bf16_f32 v120, v48, v49\n\t"
"v_cvt_pk_bf16_f32 v121, v50, v51\n\t"
"v_mfma_f32_32x32x16_bf16 v[192:207], v[140:143], v[116:119], v[192:207]\n\t"
"v_exp_f32 v56, v56\n\t"
"v_exp_f32 v57, v57\n\t"
"v_cvt_pk_bf16_f32 v122, v52, v53\n\t"
"v_cvt_pk_bf16_f32 v123, v54, v55\n\t"
"v_smfmac_f32_16x16x64_bf16 v[224:227], v[152:155], v[112:119], v158\n\t"
"v_exp_f32 v58, v58\n\t"
"v_exp_f32 v59, v59\n\t"
"v_exp_f32 v60, v60\n\t"
"v_exp_f32 v61, v61\n\t"
"v_exp_f32 v62, v62\n\t"
"v_exp_f32 v63, v63\n\t"
"v_cvt_pk_bf16_f32 v124, v56, v57\n\t"
"v_cvt_pk_bf16_f32 v125, v58, v59\n\t"
"v_cvt_pk_bf16_f32 v126, v60, v61\n\t"
"v_cvt_pk_bf16_f32 v127, v62, v63\n\t"
"v_mfma_f32_32x32x16_bf16 v[48:63], v[128:131], v[104:107], v[80:95]\n\t"
"ds_read_b128 v[128:131], v156 offset:20480\n\t"
"v_exp_f32 v32, v32\n\t"
"v_exp_f32 v33, v33\n\t"
"v_exp_f32 v34, v34\n\t"
"v_mfma_f32_32x32x16_bf16 v[208:223], v[136:139], v[120:123], v[208:223]\n\t"
"ds_read_b128 v[136:139], v156 offset:18432\n\t"
"v_exp_f32 v35, v35\n\t"
"v_exp_f32 v36, v36\n\t"
"v_exp_f32 v37, v37\n\t"
"v_mfma_f32_32x32x16_bf16 v[48:63], v[132:135], v[108:111], v[48:63]\n\t"
"ds_read_b128 v[132:135], v156 offset:21504\n\t"
"v_exp_f32 v38, v38\n\t"
"v_exp_f32 v39, v39\n\t"
"v_cvt_pk_bf16_f32 v112, v32, v33\n\t"
"v_cvt_pk_bf16_f32 v113, v34, v35\n\t"
"v_mfma_f32_32x32x16_bf16 v[208:223], v[140:143], v[124:127], v[208:223]\n\t"
"ds_read_b128 v[140:143], v156 offset:19456\n\t"
"v_exp_f32 v40, v40\n\t"
"v_exp_f32 v41, v41\n\t"
"v_cvt_pk_bf16_f32 v114, v36, v37\n\t"
"v_cvt_pk_bf16_f32 v115, v38, v39\n\t"
"v_smfmac_f32_16x16x64_bf16 v[228:231], v[152:155], v[120:127], v158\n\t"
"s_waitcnt lgkmcnt(1)\n\t"
"v_exp_f32 v42, v42\n\t"
"v_exp_f32 v43, v43\n\t"
"v_exp_f32 v44, v44\n\t"
"v_exp_f32 v45, v45\n\t"
"v_exp_f32 v46, v46\n\t"
"v_exp_f32 v47, v47\n\t"
"v_cvt_pk_bf16_f32 v116, v40, v41\n\t"
"v_cvt_pk_bf16_f32 v117, v42, v43\n\t"
"v_cvt_pk_bf16_f32 v118, v44, v45\n\t"
"v_cvt_pk_bf16_f32 v119, v46, v47\n\t"
"v_mfma_f32_32x32x16_bf16 v[32:47], v[128:131], v[96:99], v[64:79]\n\t"
"s_waitcnt lgkmcnt(0)\n\t"
"v_exp_f32 v48, v48\n\t"
"v_exp_f32 v49, v49\n\t"
"v_exp_f32 v50, v50\n\t"
"v_mfma_f32_32x32x16_bf16 v[192:207], v[136:139], v[112:115], v[192:207]\n\t"
"v_exp_f32 v51, v51\n\t"
"v_exp_f32 v52, v52\n\t"
"v_exp_f32 v53, v53\n\t"
"v_mfma_f32_32x32x16_bf16 v[32:47], v[132:135], v[100:103], v[32:47]\n\t"
"v_exp_f32 v54, v54\n\t"
"v_exp_f32 v55, v55\n\t"
"v_cvt_pk_bf16_f32 v120, v48, v49\n\t"
"v_cvt_pk_bf16_f32 v121, v50, v51\n\t"
"v_mfma_f32_32x32x16_bf16 v[192:207], v[140:143], v[116:119], v[192:207]\n\t"
"v_exp_f32 v56, v56\n\t"
"v_exp_f32 v57, v57\n\t"
"v_cvt_pk_bf16_f32 v122, v52, v53\n\t"
"v_cvt_pk_bf16_f32 v123, v54, v55\n\t"
"v_smfmac_f32_16x16x64_bf16 v[224:227], v[152:155], v[112:119], v158\n\t"
"v_exp_f32 v58, v58\n\t"
"v_exp_f32 v59, v59\n\t"
"v_exp_f32 v60, v60\n\t"
"v_exp_f32 v61, v61\n\t"
"v_exp_f32 v62, v62\n\t"
"v_exp_f32 v63, v63\n\t"
"v_cvt_pk_bf16_f32 v124, v56, v57\n\t"
"v_cvt_pk_bf16_f32 v125, v58, v59\n\t"
"v_cvt_pk_bf16_f32 v126, v60, v61\n\t"
"v_cvt_pk_bf16_f32 v127, v62, v63\n\t"
"v_mfma_f32_32x32x16_bf16 v[48:63], v[128:131], v[104:107], v[80:95]\n\t"
"ds_read_b128 v[128:131], v156 offset:24576\n\t"
"v_exp_f32 v32, v32\n\t"
"v_exp_f32 v33, v33\n\t"
"v_exp_f32 v34, v34\n\t"
"v_mfma_f32_32x32x16_bf16 v[208:223], v[136:139], v[120:123], v[208:223]\n\t"
"ds_read_b128 v[136:139], v156 offset:22528\n\t"
"v_exp_f32 v35, v35\n\t"
"v_exp_f32 v36, v36\n\t"
"v_exp_f32 v37, v37\n\t"
"v_mfma_f32_32x32x16_bf16 v[48:63], v[132:135], v[108:111], v[48:63]\n\t"
"ds_read_b128 v[132:135], v156 offset:25600\n\t"
"v_exp_f32 v38, v38\n\t"
"v_exp_f32 v39, v39\n\t"
"v_cvt_pk_bf16_f32 v112, v32, v33\n\t"
"v_cvt_pk_bf16_f32 v113, v34, v35\n\t"
"v_mfma_f32_32x32x16_bf16 v[208:223], v[140:143], v[124:127], v[208:223]\n\t"
"ds_read_b128 v[140:143], v156 offset:23552\n\t"
"v_exp_f32 v40, v40\n\t"
"v_exp_f32 v41, v41\n\t"
"v_cvt_pk_bf16_f32 v114, v36, v37\n\t"
"v_cvt_pk_bf16_f32 v115, v38, v39\n\t"
"v_smfmac_f32_16x16x64_bf16 v[228:231], v[152:155], v[120:127], v158\n\t"
"s_waitcnt lgkmcnt(1)\n\t"
"v_exp_f32 v42, v42\n\t"
"v_exp_f32 v43, v43\n\t"
"v_exp_f32 v44, v44\n\t"
"v_exp_f32 v45, v45\n\t"
"v_exp_f32 v46, v46\n\t"
"v_exp_f32 v47, v47\n\t"
"v_cvt_pk_bf16_f32 v116, v40, v41\n\t"
"v_cvt_pk_bf16_f32 v117, v42, v43\n\t"
"v_cvt_pk_bf16_f32 v118, v44, v45\n\t"
"v_cvt_pk_bf16_f32 v119, v46, v47\n\t"
"v_mfma_f32_32x32x16_bf16 v[32:47], v[128:131], v[96:99], v[64:79]\n\t"
"s_waitcnt lgkmcnt(0)\n\t"
"v_exp_f32 v48, v48\n\t"
"v_exp_f32 v49, v49\n\t"
"v_exp_f32 v50, v50\n\t"
"v_mfma_f32_32x32x16_bf16 v[192:207], v[136:139], v[112:115], v[192:207]\n\t"
"v_exp_f32 v51, v51\n\t"
"v_exp_f32 v52, v52\n\t"
"v_exp_f32 v53, v53\n\t"
"v_mfma_f32_32x32x16_bf16 v[32:47], v[132:135], v[100:103], v[32:47]\n\t"
"v_exp_f32 v54, v54\n\t"
"v_exp_f32 v55, v55\n\t"
"v_cvt_pk_bf16_f32 v120, v48, v49\n\t"
"v_cvt_pk_bf16_f32 v121, v50, v51\n\t"
"v_mfma_f32_32x32x16_bf16 v[192:207], v[140:143], v[116:119], v[192:207]\n\t"
"v_exp_f32 v56, v56\n\t"
"v_exp_f32 v57, v57\n\t"
"v_cvt_pk_bf16_f32 v122, v52, v53\n\t"
"v_cvt_pk_bf16_f32 v123, v54, v55\n\t"
"v_smfmac_f32_16x16x64_bf16 v[224:227], v[152:155], v[112:119], v158\n\t"
"v_exp_f32 v58, v58\n\t"
"v_exp_f32 v59, v59\n\t"
"v_exp_f32 v60, v60\n\t"
"v_exp_f32 v61, v61\n\t"
"v_exp_f32 v62, v62\n\t"
"v_exp_f32 v63, v63\n\t"
"v_cvt_pk_bf16_f32 v124, v56, v57\n\t"
"v_cvt_pk_bf16_f32 v125, v58, v59\n\t"
"v_cvt_pk_bf16_f32 v126, v60, v61\n\t"
"v_cvt_pk_bf16_f32 v127, v62, v63\n\t"
"v_mfma_f32_32x32x16_bf16 v[48:63], v[128:131], v[104:107], v[80:95]\n\t"
"ds_read_b128 v[128:131], v156 offset:28672\n\t"
"v_exp_f32 v32, v32\n\t"
"v_exp_f32 v33, v33\n\t"
"v_exp_f32 v34, v34\n\t"
"v_mfma_f32_32x32x16_bf16 v[208:223], v[136:139], v[120:123], v[208:223]\n\t"
"ds_read_b128 v[136:139], v156 offset:26624\n\t"
"v_exp_f32 v35, v35\n\t"
"v_exp_f32 v36, v36\n\t"
"v_exp_f32 v37, v37\n\t"
"v_mfma_f32_32x32x16_bf16 v[48:63], v[132:135], v[108:111], v[48:63]\n\t"
"ds_read_b128 v[132:135], v156 offset:29696\n\t"
"v_exp_f32 v38, v38\n\t"
"v_exp_f32 v39, v39\n\t"
"v_cvt_pk_bf16_f32 v112, v32, v33\n\t"
"v_cvt_pk_bf16_f32 v113, v34, v35\n\t"
"v_mfma_f32_32x32x16_bf16 v[208:223], v[140:143], v[124:127], v[208:223]\n\t"
"ds_read_b128 v[140:143], v156 offset:27648\n\t"
"v_exp_f32 v40, v40\n\t"
"v_exp_f32 v41, v41\n\t"
"v_cvt_pk_bf16_f32 v114, v36, v37\n\t"
"v_cvt_pk_bf16_f32 v115, v38, v39\n\t"
"v_smfmac_f32_16x16x64_bf16 v[228:231], v[152:155], v[120:127], v158\n\t"
"s_waitcnt lgkmcnt(1)\n\t"
"v_exp_f32 v42, v42\n\t"
"v_exp_f32 v43, v43\n\t"
"v_exp_f32 v44, v44\n\t"
"v_exp_f32 v45, v45\n\t"
"v_exp_f32 v46, v46\n\t"
"v_exp_f32 v47, v47\n\t"
"v_cvt_pk_bf16_f32 v116, v40, v41\n\t"
"v_cvt_pk_bf16_f32 v117, v42, v43\n\t"
"v_cvt_pk_bf16_f32 v118, v44, v45\n\t"
"v_cvt_pk_bf16_f32 v119, v46, v47\n\t"
"v_mfma_f32_32x32x16_bf16 v[32:47], v[128:131], v[96:99], v[64:79]\n\t"
"s_waitcnt lgkmcnt(0)\n\t"
"v_exp_f32 v48, v48\n\t"
"v_exp_f32 v49, v49\n\t"
"v_exp_f32 v50, v50\n\t"
"v_mfma_f32_32x32x16_bf16 v[192:207], v[136:139], v[112:115], v[192:207]\n\t"
"s_add_u32 s73, s73, 32768\n\t"
"v_exp_f32 v51, v51\n\t"
"v_exp_f32 v52, v52\n\t"
"v_exp_f32 v53, v53\n\t"
"v_mfma_f32_32x32x16_bf16 v[32:47], v[132:135], v[100:103], v[32:47]\n\t"
"s_and_b32 s73, s73, 98304\n\t"
"v_exp_f32 v54, v54\n\t"
"v_exp_f32 v55, v55\n\t"
"v_cvt_pk_bf16_f32 v120, v48, v49\n\t"
"v_cvt_pk_bf16_f32 v121, v50, v51\n\t"
"v_mfma_f32_32x32x16_bf16 v[192:207], v[140:143], v[116:119], v[192:207]\n\t"
"v_exp_f32 v56, v56\n\t"
"v_exp_f32 v57, v57\n\t"
"v_cvt_pk_bf16_f32 v122, v52, v53\n\t"
"v_cvt_pk_bf16_f32 v123, v54, v55\n\t"
"v_smfmac_f32_16x16x64_bf16 v[224:227], v[152:155], v[112:119], v158\n\t"
"s_barrier\n\t"
"v_exp_f32 v58, v58\n\t"
"v_exp_f32 v59, v59\n\t"
"v_exp_f32 v60, v60\n\t"
"v_exp_f32 v61, v61\n\t"
"v_exp_f32 v62, v62\n\t"
"v_exp_f32 v63, v63\n\t"
"v_cvt_pk_bf16_f32 v124, v56, v57\n\t"
"v_cvt_pk_bf16_f32 v125, v58, v59\n\t"
"v_cvt_pk_bf16_f32 v126, v60, v61\n\t"
"v_cvt_pk_bf16_f32 v127, v62, v63\n\t"
"v_add_u32 v157, s73, v238\n\t"
"v_mfma_f32_32x32x16_bf16 v[48:63], v[128:131], v[104:107], v[80:95]\n\t"
"ds_read_b128 v[128:131], v157 offset:0\n\t"
"v_exp_f32 v32, v32\n\t"
"v_exp_f32 v33, v33\n\t"
"v_exp_f32 v34, v34\n\t"
"v_mfma_f32_32x32x16_bf16 v[208:223], v[136:139], v[120:123], v[208:223]\n\t"
"ds_read_b128 v[136:139], v156 offset:30720\n\t"
"v_exp_f32 v35, v35\n\t"
"v_exp_f32 v36, v36\n\t"
"v_exp_f32 v37, v37\n\t"
"v_mfma_f32_32x32x16_bf16 v[48:63], v[132:135], v[108:111], v[48:63]\n\t"
"ds_read_b128 v[132:135], v157 offset:1024\n\t"
"v_exp_f32 v38, v38\n\t"
"v_exp_f32 v39, v39\n\t"
"v_cvt_pk_bf16_f32 v112, v32, v33\n\t"
"v_cvt_pk_bf16_f32 v113, v34, v35\n\t"
"v_mfma_f32_32x32x16_bf16 v[208:223], v[140:143], v[124:127], v[208:223]\n\t"
"ds_read_b128 v[140:143], v156 offset:31744\n\t"
"v_exp_f32 v40, v40\n\t"
"v_exp_f32 v41, v41\n\t"
"v_cvt_pk_bf16_f32 v114, v36, v37\n\t"
"v_cvt_pk_bf16_f32 v115, v38, v39\n\t"
"v_smfmac_f32_16x16x64_bf16 v[228:231], v[152:155], v[120:127], v158\n\t"
"s_waitcnt lgkmcnt(1)\n\t"
"v_exp_f32 v42, v42\n\t"
"v_exp_f32 v43, v43\n\t"
"v_exp_f32 v44, v44\n\t"
"v_exp_f32 v45, v45\n\t"
"v_exp_f32 v46, v46\n\t"
"v_exp_f32 v47, v47\n\t"
"v_cvt_pk_bf16_f32 v116, v40, v41\n\t"
"v_cvt_pk_bf16_f32 v117, v42, v43\n\t"
"v_cvt_pk_bf16_f32 v118, v44, v45\n\t"
"v_cvt_pk_bf16_f32 v119, v46, v47\n\t"
"v_mov_b32 v156, v157\n\t"
"v_mfma_f32_32x32x16_bf16 v[32:47], v[128:131], v[96:99], v[64:79]\n\t"
"s_waitcnt lgkmcnt(0)\n\t"
"v_exp_f32 v48, v48\n\t"
"v_exp_f32 v49, v49\n\t"
"v_exp_f32 v50, v50\n\t"
"v_mfma_f32_32x32x16_bf16 v[192:207], v[136:139], v[112:115], v[192:207]\n\t"
"s_add_u32 s72, s72, 1\n\t"
"v_exp_f32 v51, v51\n\t"
"v_exp_f32 v52, v52\n\t"
"v_exp_f32 v53, v53\n\t"
"v_mfma_f32_32x32x16_bf16 v[32:47], v[132:135], v[100:103], v[32:47]\n\t"
"s_cmp_lg_u32 s72, 8\n\t"
"v_exp_f32 v54, v54\n\t"
"v_exp_f32 v55, v55\n\t"
"v_cvt_pk_bf16_f32 v120, v48, v49\n\t"
"v_cvt_pk_bf16_f32 v121, v50, v51\n\t"
"v_mfma_f32_32x32x16_bf16 v[192:207], v[140:143], v[116:119], v[192:207]\n\t"
"v_exp_f32 v56, v56\n\t"
"v_exp_f32 v57, v57\n\t"
"v_cvt_pk_bf16_f32 v122, v52, v53\n\t"
"v_cvt_pk_bf16_f32 v123, v54, v55\n\t"
"v_smfmac_f32_16x16x64_bf16 v[224:227], v[152:155], v[112:119], v158\n\t"
"v_exp_f32 v58, v58\n\t"
"v_exp_f32 v59, v59\n\t"
"v_exp_f32 v60, v60\n\t"
"v_exp_f32 v61, v61\n\t"
"v_exp_f32 v62, v62\n\t"
"v_exp_f32 v63, v63\n\t"
"v_cvt_pk_bf16_f32 v124, v56, v57\n\t"
"v_cvt_pk_bf16_f32 v125, v58, v59\n\t"
"v_cvt_pk_bf16_f32 v126, v60, v61\n\t"
"v_cvt_pk_bf16_f32 v127, v62, v63\n\t"
"v_mfma_f32_32x32x16_bf16 v[48:63], v[128:131], v[104:107], v[80:95]\n\t"
"s_cbranch_scc1 L_att_%=\n\t"
"s_waitcnt vmcnt(0) lgkmcnt(0)\n\t"
"v_mfma_f32_32x32x16_bf16 v[208:223], v[136:139], v[120:123], v[208:223]\n\t"
"v_mfma_f32_32x32x16_bf16 v[208:223], v[140:143], v[124:127], v[208:223]\n\t"
"v_smfmac_f32_16x16x64_bf16 v[228:231], v[152:155], v[120:127], v158\n\t"
"s_barrier\n\t"
"ds_read_b128 v[160:163], v250 offset:32768\n\t"
"ds_read_b128 v[164:167], v250 offset:34816\n\t"
"ds_read_b128 v[168:171], v250 offset:40960\n\t"
"ds_read_b128 v[172:175], v250 offset:43008\n\t"
"ds_read_b128 v[128:131], v248 offset:0\n\t"
"ds_read_b128 v[132:135], v248 offset:2048\n\t"
"ds_read_b128 v[136:139], v248 offset:4096\n\t"
"ds_read_b128 v[140:143], v248 offset:6144\n\t"
"ds_read_b128 v[144:147], v248 offset:16384\n\t"
"ds_read_b128 v[148:151], v248 offset:18432\n\t"
"ds_read_b128 v[152:155], v248 offset:20480\n\t"
"ds_read_b128 v[156:159], v248 offset:22528\n\t"
"v_and_b32 v240, 16, v252\n\t"
"v_cmp_ne_u32 vcc, 0, v240\n\t"
"v_mul_f32 v240, 0, v192\n\t"
"v_mul_f32 v241, 0, v193\n\t"
"v_fmac_f32 v240, 0, v194\n\t"
"v_fmac_f32 v241, 0, v195\n\t"
"v_fmac_f32 v240, 0, v196\n\t"
"v_fmac_f32 v241, 0, v197\n\t"
"v_fmac_f32 v240, 0, v198\n\t"
"v_fmac_f32 v241, 0, v199\n\t"
"v_fmac_f32 v240, 0, v200\n\t"
"v_fmac_f32 v241, 0, v201\n\t"
"v_fmac_f32 v240, 0, v202\n\t"
"v_fmac_f32 v241, 0, v203\n\t"
"v_fmac_f32 v240, 0, v204\n\t"
"v_fmac_f32 v241, 0, v205\n\t"
"v_fmac_f32 v240, 0, v206\n\t"
"v_fmac_f32 v241, 0, v207\n\t"
"v_cndmask_b32 v242, v224, v225, vcc\n\t"
"v_cndmask_b32 v243, v228, v229, vcc\n\t"
"v_cndmask_b32 v224, v226, v227, vcc\n\t"
"v_cndmask_b32 v228, v230, v231, vcc\n\t"
"v_add_f32 v242, v242, v224\n\t"
"v_add_f32 v243, v243, v228\n\t"
"v_fmac_f32 v240, 0, v208\n\t"
"v_fmac_f32 v241, 0, v209\n\t"
"v_fmac_f32 v240, 0, v210\n\t"
"v_fmac_f32 v241, 0, v211\n\t"
"v_fmac_f32 v240, 0, v212\n\t"
"v_fmac_f32 v241, 0, v213\n\t"
"v_fmac_f32 v240, 0, v214\n\t"
"v_fmac_f32 v241, 0, v215\n\t"
"v_fmac_f32 v240, 0, v216\n\t"
"v_fmac_f32 v241, 0, v217\n\t"
"v_fmac_f32 v240, 0, v218\n\t"
"v_fmac_f32 v241, 0, v219\n\t"
"v_fmac_f32 v240, 0, v220\n\t"
"v_fmac_f32 v241, 0, v221\n\t"
"v_fmac_f32 v240, 0, v222\n\t"
"v_fmac_f32 v241, 0, v223\n\t"
"v_fmac_f32 v240, 0, v242\n\t"
"v_fmac_f32 v241, 0, v243\n\t"
"v_cmp_u_f32 vcc, v240, v241\n\t"
"s_nop 4\n\t"
"s_cbranch_vccnz L_fallback_%=\n\t"
"L_att_ok_%=:\n\t"
"s_setprio 1\n\t"
"v_add_u32 v228, 0x10000, v248\n\t"
"v_add_u32 v229, 0x10000, v249\n\t"
"s_waitcnt lgkmcnt(7)\n\t"
"v_mfma_f32_16x16x32_bf16 v[0:3], v[160:163], v[128:131], 0\n\t"
"v_mul_f32 v242, v242, v236\n\t"
"s_and_b32 s59, s45, 1\n\t"
"ds_read_b128 v[176:179], v250 offset:33792\n\t"
"s_waitcnt lgkmcnt(7)\n\t"
"v_mfma_f32_16x16x32_bf16 v[16:19], v[160:163], v[132:135], 0\n\t"
"v_mul_f32 v243, v243, v237\n\t"
"s_lshl_b32 s59, s59, 14\n\t"
"ds_read_b128 v[180:183], v250 offset:35840\n\t"
"v_mfma_f32_16x16x32_bf16 v[20:23], v[164:167], v[132:135], 0\n\t"
"v_rcp_f32 v242, v242\n\t"
"s_lshr_b32 s60, s45, 1\n\t"
"ds_read_b128 v[184:187], v250 offset:41984\n\t"
"v_mfma_f32_16x16x32_bf16 v[4:7], v[164:167], v[128:131], 0\n\t"
"v_rcp_f32 v243, v243\n\t"
"s_lshl_b32 s60, s60, 11\n\t"
"ds_read_b128 v[188:191], v250 offset:44032\n\t"
"v_mfma_f32_16x16x32_bf16 v[8:11], v[168:171], v[128:131], 0\n\t"
"v_sub_f32 v236, 1.0, v236\n\t"
"s_add_u32 s59, s59, s60\n\t"
"v_mfma_f32_16x16x32_bf16 v[24:27], v[168:171], v[132:135], 0\n\t"
"v_sub_f32 v237, 1.0, v237\n\t"
"s_add_u32 s59, s59, 32768\n\t"
"v_mfma_f32_16x16x32_bf16 v[28:31], v[172:175], v[132:135], 0\n\t"
"v_mul_f32 v242, v242, v236\n\t"
"v_add_u32 v226, s59, v238\n\t"
"v_mfma_f32_16x16x32_bf16 v[12:15], v[172:175], v[128:131], 0\n\t"
"v_mul_f32 v243, v243, v237\n\t"
"s_lshl_b32 s60, s45, 12\n\t"
"s_waitcnt lgkmcnt(4)\n\t"
"v_mfma_f32_16x16x32_bf16 v[44:47], v[172:175], v[136:139], 0\n\t"
"v_mul_f32 v192, v242, v192\n\t"
"v_add_u32 v227, s60, v238\n\t"
"ds_read_b128 v[128:131], v249 offset:0\n\t"
"v_mfma_f32_16x16x32_bf16 v[60:63], v[172:175], v[140:143], 0\n\t"
"v_mul_f32 v193, v242, v193\n\t"
"v_and_b32 v224, 1, v254\n\t"
"ds_read_b128 v[132:135], v249 offset:2048\n\t"
"v_mfma_f32_16x16x32_bf16 v[56:59], v[168:171], v[140:143], 0\n\t"
"v_mul_f32 v194, v242, v194\n\t"
"v_lshlrev_b32 v224, 5, v224\n\t"
"v_mfma_f32_16x16x32_bf16 v[40:43], v[168:171], v[136:139], 0\n\t"
"v_mul_f32 v195, v242, v195\n\t"
"v_lshrrev_b32 v241, 1, v254\n\t"
"v_mfma_f32_16x16x32_bf16 v[36:39], v[164:167], v[136:139], 0\n\t"
"v_mul_f32 v196, v242, v196\n\t"
"v_lshl_add_u32 v224, v241, 4, v224\n\t"
"v_mfma_f32_16x16x32_bf16 v[52:55], v[164:167], v[140:143], 0\n\t"
"v_mul_f32 v197, v242, v197\n\t"
"v_lshl_add_u32 v224, v253, 11, v224\n\t"
"v_mfma_f32_16x16x32_bf16 v[48:51], v[160:163], v[140:143], 0\n\t"
"v_mul_f32 v198, v242, v198\n\t"
"v_and_b32 v225, 3, v252\n\t"
"v_mfma_f32_16x16x32_bf16 v[32:35], v[160:163], v[136:139], 0\n\t"
"v_mul_f32 v199, v242, v199\n\t"
"v_min_u32 v225, 2, v225\n\t"
"v_mfma_f32_16x16x32_bf16 v[64:67], v[160:163], v[144:147], 0\n\t"
"v_cvt_pk_bf16_f32 v192, v192, v193\n\t"
"v_lshlrev_b32 v225, 14, v225\n\t"
"ds_read_b128 v[136:139], v249 offset:4096\n\t"
"v_mfma_f32_16x16x32_bf16 v[80:83], v[160:163], v[148:151], 0\n\t"
"v_cvt_pk_bf16_f32 v193, v194, v195\n\t"
"s_add_u32 s60, s60, 1020\n\t"
"ds_read_b128 v[140:143], v249 offset:6144\n\t"
"v_mfma_f32_16x16x32_bf16 v[84:87], v[164:167], v[148:151], 0\n\t"
"v_cvt_pk_bf16_f32 v194, v196, v197\n\t"
"v_add_u32 v225, s60, v225\n\t"
"v_mfma_f32_16x16x32_bf16 v[68:71], v[164:167], v[144:147], 0\n\t"
"v_cvt_pk_bf16_f32 v195, v198, v199\n\t"
"v_mfma_f32_16x16x32_bf16 v[72:75], v[168:171], v[144:147], 0\n\t"
"buffer_store_dwordx4 v[192:195], v251, s[24:27], s98 offen sc1\n\t"
"v_mfma_f32_16x16x32_bf16 v[88:91], v[168:171], v[148:151], 0\n\t"
"v_mul_f32 v200, v242, v200\n\t"
"v_mfma_f32_16x16x32_bf16 v[92:95], v[172:175], v[148:151], 0\n\t"
"v_mul_f32 v201, v242, v201\n\t"
"v_mfma_f32_16x16x32_bf16 v[76:79], v[172:175], v[144:147], 0\n\t"
"v_mul_f32 v202, v242, v202\n\t"
"v_mfma_f32_16x16x32_bf16 v[108:111], v[172:175], v[152:155], 0\n\t"
"v_mul_f32 v203, v242, v203\n\t"
"ds_read_b128 v[144:147], v249 offset:16384\n\t"
"v_mfma_f32_16x16x32_bf16 v[124:127], v[172:175], v[156:159], 0\n\t"
"v_mul_f32 v204, v242, v204\n\t"
"ds_read_b128 v[148:151], v249 offset:18432\n\t"
"v_mfma_f32_16x16x32_bf16 v[120:123], v[168:171], v[156:159], 0\n\t"
"v_mul_f32 v205, v242, v205\n\t"
"v_mfma_f32_16x16x32_bf16 v[104:107], v[168:171], v[152:155], 0\n\t"
"v_mul_f32 v206, v242, v206\n\t"
"v_mfma_f32_16x16x32_bf16 v[100:103], v[164:167], v[152:155], 0\n\t"
"v_mul_f32 v207, v242, v207\n\t"
"v_mfma_f32_16x16x32_bf16 v[116:119], v[164:167], v[156:159], 0\n\t"
"v_cvt_pk_bf16_f32 v200, v200, v201\n\t"
"v_mfma_f32_16x16x32_bf16 v[112:115], v[160:163], v[156:159], 0\n\t"
"v_cvt_pk_bf16_f32 v201, v202, v203\n\t"
"v_mfma_f32_16x16x32_bf16 v[96:99], v[160:163], v[152:155], 0\n\t"
"v_cvt_pk_bf16_f32 v202, v204, v205\n\t"
"ds_read_b128 v[152:155], v249 offset:20480\n\t"
"s_waitcnt lgkmcnt(3)\n\t"
"v_mfma_f32_16x16x32_bf16 v[0:3], v[176:179], v[128:131], v[0:3]\n\t"
"v_cvt_pk_bf16_f32 v203, v206, v207\n\t"
"ds_read_b128 v[156:159], v249 offset:22528\n\t"
"v_mfma_f32_16x16x32_bf16 v[16:19], v[176:179], v[132:135], v[16:19]\n\t"
"buffer_store_dwordx4 v[200:203], v251, s[24:27], s98 offen offset:512 sc1\n\t"
"v_mfma_f32_16x16x32_bf16 v[20:23], v[180:183], v[132:135], v[20:23]\n\t"
"v_mul_f32 v208, v243, v208\n\t"
"v_mfma_f32_16x16x32_bf16 v[4:7], v[180:183], v[128:131], v[4:7]\n\t"
"v_mul_f32 v209, v243, v209\n\t"
"v_mfma_f32_16x16x32_bf16 v[8:11], v[184:187], v[128:131], v[8:11]\n\t"
"v_mul_f32 v210, v243, v210\n\t"
"v_mfma_f32_16x16x32_bf16 v[24:27], v[184:187], v[132:135], v[24:27]\n\t"
"v_mul_f32 v211, v243, v211\n\t"
"v_mfma_f32_16x16x32_bf16 v[28:31], v[188:191], v[132:135], v[28:31]\n\t"
"v_mul_f32 v212, v243, v212\n\t"
"v_mfma_f32_16x16x32_bf16 v[12:15], v[188:191], v[128:131], v[12:15]\n\t"
"v_mul_f32 v213, v243, v213\n\t"
"s_waitcnt lgkmcnt(0)\n\t"
"s_barrier\n\t"
"v_mfma_f32_16x16x32_bf16 v[44:47], v[188:191], v[136:139], v[44:47]\n\t"
"v_mul_f32 v214, v243, v214\n\t"
"ds_read_b128 v[160:163], v250 offset:49152\n\t"
"v_mfma_f32_16x16x32_bf16 v[60:63], v[188:191], v[140:143], v[60:63]\n\t"
"v_mul_f32 v215, v243, v215\n\t"
"ds_read_b128 v[164:167], v250 offset:51200\n\t"
"v_mfma_f32_16x16x32_bf16 v[56:59], v[184:187], v[140:143], v[56:59]\n\t"
"v_cvt_pk_bf16_f32 v208, v208, v209\n\t"
"ds_read_b128 v[168:171], v250 offset:57344\n\t"
"v_mfma_f32_16x16x32_bf16 v[40:43], v[184:187], v[136:139], v[40:43]\n\t"
"v_cvt_pk_bf16_f32 v209, v210, v211\n\t"
"ds_read_b128 v[172:175], v250 offset:59392\n\t"
"v_mfma_f32_16x16x32_bf16 v[36:39], v[180:183], v[136:139], v[36:39]\n\t"
"v_cvt_pk_bf16_f32 v210, v212, v213\n\t"
"ds_read_b128 v[128:131], v248 offset:32768\n\t"
"v_mfma_f32_16x16x32_bf16 v[52:55], v[180:183], v[140:143], v[52:55]\n\t"
"v_cvt_pk_bf16_f32 v211, v214, v215\n\t"
"ds_read_b128 v[132:135], v248 offset:34816\n\t"
"v_mfma_f32_16x16x32_bf16 v[48:51], v[176:179], v[140:143], v[48:51]\n\t"
"buffer_store_dwordx4 v[208:211], v251, s[24:27], s99 offen sc1\n\t"
"v_mfma_f32_16x16x32_bf16 v[32:35], v[176:179], v[136:139], v[32:35]\n\t"
"v_mul_f32 v216, v243, v216\n\t"
"v_mfma_f32_16x16x32_bf16 v[64:67], v[176:179], v[144:147], v[64:67]\n\t"
"v_mul_f32 v217, v243, v217\n\t"
"ds_read_b128 v[136:139], v248 offset:36864\n\t"
"v_mfma_f32_16x16x32_bf16 v[80:83], v[176:179], v[148:151], v[80:83]\n\t"
"v_mul_f32 v218, v243, v218\n\t"
"ds_read_b128 v[140:143], v248 offset:38912\n\t"
"v_mfma_f32_16x16x32_bf16 v[84:87], v[180:183], v[148:151], v[84:87]\n\t"
"v_mul_f32 v219, v243, v219\n\t"
"v_mfma_f32_16x16x32_bf16 v[68:71], v[180:183], v[144:147], v[68:71]\n\t"
"v_mul_f32 v220, v243, v220\n\t"
"v_mfma_f32_16x16x32_bf16 v[72:75], v[184:187], v[144:147], v[72:75]\n\t"
"v_mul_f32 v221, v243, v221\n\t"
"v_mfma_f32_16x16x32_bf16 v[88:91], v[184:187], v[148:151], v[88:91]\n\t"
"v_mul_f32 v222, v243, v222\n\t"
"v_mfma_f32_16x16x32_bf16 v[92:95], v[188:191], v[148:151], v[92:95]\n\t"
"v_mul_f32 v223, v243, v223\n\t"
"v_mfma_f32_16x16x32_bf16 v[76:79], v[188:191], v[144:147], v[76:79]\n\t"
"v_cvt_pk_bf16_f32 v216, v216, v217\n\t"
"v_mfma_f32_16x16x32_bf16 v[108:111], v[188:191], v[152:155], v[108:111]\n\t"
"v_cvt_pk_bf16_f32 v217, v218, v219\n\t"
"ds_read_b128 v[144:147], v248 offset:49152\n\t"
"v_mfma_f32_16x16x32_bf16 v[124:127], v[188:191], v[156:159], v[124:127]\n\t"
"v_cvt_pk_bf16_f32 v218, v220, v221\n\t"
"ds_read_b128 v[148:151], v248 offset:51200\n\t"
"v_mfma_f32_16x16x32_bf16 v[120:123], v[184:187], v[156:159], v[120:123]\n\t"
"v_cvt_pk_bf16_f32 v219, v222, v223\n\t"
"v_mfma_f32_16x16x32_bf16 v[104:107], v[184:187], v[152:155], v[104:107]\n\t"
"buffer_store_dwordx4 v[216:219], v251, s[24:27], s99 offen offset:512 sc1\n\t"
"v_mfma_f32_16x16x32_bf16 v[100:103], v[180:183], v[152:155], v[100:103]\n\t"
"v_mfma_f32_16x16x32_bf16 v[116:119], v[180:183], v[156:159], v[116:119]\n\t"
"v_mfma_f32_16x16x32_bf16 v[112:115], v[176:179], v[156:159], v[112:115]\n\t"
"v_mfma_f32_16x16x32_bf16 v[96:99], v[176:179], v[152:155], v[96:99]\n\t"
"ds_read_b128 v[152:155], v248 offset:53248\n\t"
"s_mov_b32 s56, 3\n\t"
".p2align 6\n\t"
"s_nop 0\n\t"
"s_nop 0\n\t"
"L_g2c_%=:\n\t"
"s_waitcnt lgkmcnt(3)\n\t"
"v_mfma_f32_16x16x32_bf16 v[0:3], v[160:163], v[128:131], v[0:3]\n\t"
"ds_read_b128 v[156:159], v248 offset:55296\n\t"
"v_mfma_f32_16x16x32_bf16 v[16:19], v[160:163], v[132:135], v[16:19]\n\t"
"ds_read_b128 v[176:179], v250 offset:50176\n\t"
"v_mfma_f32_16x16x32_bf16 v[20:23], v[164:167], v[132:135], v[20:23]\n\t"
"ds_read_b128 v[180:183], v250 offset:52224\n\t"
"v_mfma_f32_16x16x32_bf16 v[4:7], v[164:167], v[128:131], v[4:7]\n\t"
"ds_read_b128 v[184:187], v250 offset:58368\n\t"
"v_mfma_f32_16x16x32_bf16 v[8:11], v[168:171], v[128:131], v[8:11]\n\t"
"ds_read_b128 v[188:191], v250 offset:60416\n\t"
"v_mfma_f32_16x16x32_bf16 v[24:27], v[168:171], v[132:135], v[24:27]\n\t"
"v_mfma_f32_16x16x32_bf16 v[28:31], v[172:175], v[132:135], v[28:31]\n\t"
"v_mfma_f32_16x16x32_bf16 v[12:15], v[172:175], v[128:131], v[12:15]\n\t"
"v_mfma_f32_16x16x32_bf16 v[44:47], v[172:175], v[136:139], v[44:47]\n\t"
"ds_read_b128 v[128:131], v249 offset:32768\n\t"
"v_mfma_f32_16x16x32_bf16 v[60:63], v[172:175], v[140:143], v[60:63]\n\t"
"ds_read_b128 v[132:135], v249 offset:34816\n\t"
"v_mfma_f32_16x16x32_bf16 v[56:59], v[168:171], v[140:143], v[56:59]\n\t"
"v_mfma_f32_16x16x32_bf16 v[40:43], v[168:171], v[136:139], v[40:43]\n\t"
"v_mfma_f32_16x16x32_bf16 v[36:39], v[164:167], v[136:139], v[36:39]\n\t"
"v_mfma_f32_16x16x32_bf16 v[52:55], v[164:167], v[140:143], v[52:55]\n\t"
"v_mfma_f32_16x16x32_bf16 v[48:51], v[160:163], v[140:143], v[48:51]\n\t"
"v_mfma_f32_16x16x32_bf16 v[32:35], v[160:163], v[136:139], v[32:35]\n\t"
"s_waitcnt lgkmcnt(2)\n\t"
"v_mfma_f32_16x16x32_bf16 v[64:67], v[160:163], v[144:147], v[64:67]\n\t"
"ds_read_b128 v[136:139], v249 offset:36864\n\t"
"v_mfma_f32_16x16x32_bf16 v[80:83], v[160:163], v[148:151], v[80:83]\n\t"
"ds_read_b128 v[140:143], v249 offset:38912\n\t"
"v_mfma_f32_16x16x32_bf16 v[84:87], v[164:167], v[148:151], v[84:87]\n\t"
"v_mfma_f32_16x16x32_bf16 v[68:71], v[164:167], v[144:147], v[68:71]\n\t"
"v_mfma_f32_16x16x32_bf16 v[72:75], v[168:171], v[144:147], v[72:75]\n\t"
"v_mfma_f32_16x16x32_bf16 v[88:91], v[168:171], v[148:151], v[88:91]\n\t"
"v_mfma_f32_16x16x32_bf16 v[92:95], v[172:175], v[148:151], v[92:95]\n\t"
"v_mfma_f32_16x16x32_bf16 v[76:79], v[172:175], v[144:147], v[76:79]\n\t"
"v_mfma_f32_16x16x32_bf16 v[108:111], v[172:175], v[152:155], v[108:111]\n\t"
"ds_read_b128 v[144:147], v249 offset:49152\n\t"
"v_mfma_f32_16x16x32_bf16 v[124:127], v[172:175], v[156:159], v[124:127]\n\t"
"ds_read_b128 v[148:151], v249 offset:51200\n\t"
"v_mfma_f32_16x16x32_bf16 v[120:123], v[168:171], v[156:159], v[120:123]\n\t"
"v_mfma_f32_16x16x32_bf16 v[104:107], v[168:171], v[152:155], v[104:107]\n\t"
"v_mfma_f32_16x16x32_bf16 v[100:103], v[164:167], v[152:155], v[100:103]\n\t"
"v_mfma_f32_16x16x32_bf16 v[116:119], v[164:167], v[156:159], v[116:119]\n\t"
"v_mfma_f32_16x16x32_bf16 v[112:115], v[160:163], v[156:159], v[112:115]\n\t"
"v_mfma_f32_16x16x32_bf16 v[96:99], v[160:163], v[152:155], v[96:99]\n\t"
"ds_read_b128 v[152:155], v249 offset:53248\n\t"
"s_waitcnt lgkmcnt(3)\n\t"
"v_mfma_f32_16x16x32_bf16 v[0:3], v[176:179], v[128:131], v[0:3]\n\t"
"ds_read_b128 v[156:159], v249 offset:55296\n\t"
"v_mfma_f32_16x16x32_bf16 v[16:19], v[176:179], v[132:135], v[16:19]\n\t"
"v_mfma_f32_16x16x32_bf16 v[20:23], v[180:183], v[132:135], v[20:23]\n\t"
"v_mfma_f32_16x16x32_bf16 v[4:7], v[180:183], v[128:131], v[4:7]\n\t"
"v_mfma_f32_16x16x32_bf16 v[8:11], v[184:187], v[128:131], v[8:11]\n\t"
"v_mfma_f32_16x16x32_bf16 v[24:27], v[184:187], v[132:135], v[24:27]\n\t"
"v_mfma_f32_16x16x32_bf16 v[28:31], v[188:191], v[132:135], v[28:31]\n\t"
"v_mfma_f32_16x16x32_bf16 v[12:15], v[188:191], v[128:131], v[12:15]\n\t"
"s_waitcnt lgkmcnt(0)\n\t"
"s_barrier\n\t"
"v_mfma_f32_16x16x32_bf16 v[44:47], v[188:191], v[136:139], v[44:47]\n\t"
"ds_read_b128 v[160:163], v250 offset:0\n\t"
"v_mfma_f32_16x16x32_bf16 v[60:63], v[188:191], v[140:143], v[60:63]\n\t"
"ds_read_b128 v[164:167], v250 offset:2048\n\t"
"v_mfma_f32_16x16x32_bf16 v[56:59], v[184:187], v[140:143], v[56:59]\n\t"
"ds_read_b128 v[168:171], v250 offset:8192\n\t"
"v_mfma_f32_16x16x32_bf16 v[40:43], v[184:187], v[136:139], v[40:43]\n\t"
"ds_read_b128 v[172:175], v250 offset:10240\n\t"
"v_mfma_f32_16x16x32_bf16 v[36:39], v[180:183], v[136:139], v[36:39]\n\t"
"ds_read_b128 v[128:131], v228 offset:0\n\t"
"v_mfma_f32_16x16x32_bf16 v[52:55], v[180:183], v[140:143], v[52:55]\n\t"
"ds_read_b128 v[132:135], v228 offset:2048\n\t"
"v_mfma_f32_16x16x32_bf16 v[48:51], v[176:179], v[140:143], v[48:51]\n\t"
"v_mfma_f32_16x16x32_bf16 v[32:35], v[176:179], v[136:139], v[32:35]\n\t"
"v_mfma_f32_16x16x32_bf16 v[64:67], v[176:179], v[144:147], v[64:67]\n\t"
"ds_read_b128 v[136:139], v228 offset:4096\n\t"
"v_mfma_f32_16x16x32_bf16 v[80:83], v[176:179], v[148:151], v[80:83]\n\t"
"ds_read_b128 v[140:143], v228 offset:6144\n\t"
"v_mfma_f32_16x16x32_bf16 v[84:87], v[180:183], v[148:151], v[84:87]\n\t"
"v_mfma_f32_16x16x32_bf16 v[68:71], v[180:183], v[144:147], v[68:71]\n\t"
"v_mfma_f32_16x16x32_bf16 v[72:75], v[184:187], v[144:147], v[72:75]\n\t"
"v_mfma_f32_16x16x32_bf16 v[88:91], v[184:187], v[148:151], v[88:91]\n\t"
"v_mfma_f32_16x16x32_bf16 v[92:95], v[188:191], v[148:151], v[92:95]\n\t"
"v_mfma_f32_16x16x32_bf16 v[76:79], v[188:191], v[144:147], v[76:79]\n\t"
"v_mfma_f32_16x16x32_bf16 v[108:111], v[188:191], v[152:155], v[108:111]\n\t"
"ds_read_b128 v[144:147], v228 offset:16384\n\t"
"v_mfma_f32_16x16x32_bf16 v[124:127], v[188:191], v[156:159], v[124:127]\n\t"
"ds_read_b128 v[148:151], v228 offset:18432\n\t"
"v_mfma_f32_16x16x32_bf16 v[120:123], v[184:187], v[156:159], v[120:123]\n\t"
"v_mfma_f32_16x16x32_bf16 v[104:107], v[184:187], v[152:155], v[104:107]\n\t"
"v_mfma_f32_16x16x32_bf16 v[100:103], v[180:183], v[152:155], v[100:103]\n\t"
"v_mfma_f32_16x16x32_bf16 v[116:119], v[180:183], v[156:159], v[116:119]\n\t"
"v_mfma_f32_16x16x32_bf16 v[112:115], v[176:179], v[156:159], v[112:115]\n\t"
"v_mfma_f32_16x16x32_bf16 v[96:99], v[176:179], v[152:155], v[96:99]\n\t"
"ds_read_b128 v[152:155], v228 offset:20480\n\t"
"s_waitcnt lgkmcnt(3)\n\t"
"v_mfma_f32_16x16x32_bf16 v[0:3], v[160:163], v[128:131], v[0:3]\n\t"
"ds_read_b128 v[156:159], v228 offset:22528\n\t"
"v_mfma_f32_16x16x32_bf16 v[16:19], v[160:163], v[132:135], v[16:19]\n\t"
"ds_read_b128 v[176:179], v250 offset:1024\n\t"
"v_mfma_f32_16x16x32_bf16 v[20:23], v[164:167], v[132:135], v[20:23]\n\t"
"ds_read_b128 v[180:183], v250 offset:3072\n\t"
"v_mfma_f32_16x16x32_bf16 v[4:7], v[164:167], v[128:131], v[4:7]\n\t"
"ds_read_b128 v[184:187], v250 offset:9216\n\t"
"v_mfma_f32_16x16x32_bf16 v[8:11], v[168:171], v[128:131], v[8:11]\n\t"
"ds_read_b128 v[188:191], v250 offset:11264\n\t"
"v_mfma_f32_16x16x32_bf16 v[24:27], v[168:171], v[132:135], v[24:27]\n\t"
"v_mfma_f32_16x16x32_bf16 v[28:31], v[172:175], v[132:135], v[28:31]\n\t"
"v_mfma_f32_16x16x32_bf16 v[12:15], v[172:175], v[128:131], v[12:15]\n\t"
"v_mfma_f32_16x16x32_bf16 v[44:47], v[172:175], v[136:139], v[44:47]\n\t"
"ds_read_b128 v[128:131], v229 offset:0\n\t"
"v_mfma_f32_16x16x32_bf16 v[60:63], v[172:175], v[140:143], v[60:63]\n\t"
"ds_read_b128 v[132:135], v229 offset:2048\n\t"
"v_mfma_f32_16x16x32_bf16 v[56:59], v[168:171], v[140:143], v[56:59]\n\t"
"v_mfma_f32_16x16x32_bf16 v[40:43], v[168:171], v[136:139], v[40:43]\n\t"
"v_mfma_f32_16x16x32_bf16 v[36:39], v[164:167], v[136:139], v[36:39]\n\t"
"v_mfma_f32_16x16x32_bf16 v[52:55], v[164:167], v[140:143], v[52:55]\n\t"
"v_mfma_f32_16x16x32_bf16 v[48:51], v[160:163], v[140:143], v[48:51]\n\t"
"v_mfma_f32_16x16x32_bf16 v[32:35], v[160:163], v[136:139], v[32:35]\n\t"
"s_waitcnt lgkmcnt(2)\n\t"
"v_mfma_f32_16x16x32_bf16 v[64:67], v[160:163], v[144:147], v[64:67]\n\t"
"ds_read_b128 v[136:139], v229 offset:4096\n\t"
"v_mfma_f32_16x16x32_bf16 v[80:83], v[160:163], v[148:151], v[80:83]\n\t"
"ds_read_b128 v[140:143], v229 offset:6144\n\t"
"v_mfma_f32_16x16x32_bf16 v[84:87], v[164:167], v[148:151], v[84:87]\n\t"
"v_mfma_f32_16x16x32_bf16 v[68:71], v[164:167], v[144:147], v[68:71]\n\t"
"v_mfma_f32_16x16x32_bf16 v[72:75], v[168:171], v[144:147], v[72:75]\n\t"
"v_mfma_f32_16x16x32_bf16 v[88:91], v[168:171], v[148:151], v[88:91]\n\t"
"v_mfma_f32_16x16x32_bf16 v[92:95], v[172:175], v[148:151], v[92:95]\n\t"
"v_mfma_f32_16x16x32_bf16 v[76:79], v[172:175], v[144:147], v[76:79]\n\t"
"v_mfma_f32_16x16x32_bf16 v[108:111], v[172:175], v[152:155], v[108:111]\n\t"
"ds_read_b128 v[144:147], v229 offset:16384\n\t"
"v_mfma_f32_16x16x32_bf16 v[124:127], v[172:175], v[156:159], v[124:127]\n\t"
"ds_read_b128 v[148:151], v229 offset:18432\n\t"
"v_mfma_f32_16x16x32_bf16 v[120:123], v[168:171], v[156:159], v[120:123]\n\t"
"v_mfma_f32_16x16x32_bf16 v[104:107], v[168:171], v[152:155], v[104:107]\n\t"
"v_mfma_f32_16x16x32_bf16 v[100:103], v[164:167], v[152:155], v[100:103]\n\t"
"v_mfma_f32_16x16x32_bf16 v[116:119], v[164:167], v[156:159], v[116:119]\n\t"
"v_mfma_f32_16x16x32_bf16 v[112:115], v[160:163], v[156:159], v[112:115]\n\t"
"v_mfma_f32_16x16x32_bf16 v[96:99], v[160:163], v[152:155], v[96:99]\n\t"
"ds_read_b128 v[152:155], v229 offset:20480\n\t"
"s_waitcnt lgkmcnt(3)\n\t"
"v_mfma_f32_16x16x32_bf16 v[0:3], v[176:179], v[128:131], v[0:3]\n\t"
"ds_read_b128 v[156:159], v229 offset:22528\n\t"
"v_mfma_f32_16x16x32_bf16 v[16:19], v[176:179], v[132:135], v[16:19]\n\t"
"v_mfma_f32_16x16x32_bf16 v[20:23], v[180:183], v[132:135], v[20:23]\n\t"
"v_mfma_f32_16x16x32_bf16 v[4:7], v[180:183], v[128:131], v[4:7]\n\t"
"v_mfma_f32_16x16x32_bf16 v[8:11], v[184:187], v[128:131], v[8:11]\n\t"
"v_mfma_f32_16x16x32_bf16 v[24:27], v[184:187], v[132:135], v[24:27]\n\t"
"v_mfma_f32_16x16x32_bf16 v[28:31], v[188:191], v[132:135], v[28:31]\n\t"
"v_mfma_f32_16x16x32_bf16 v[12:15], v[188:191], v[128:131], v[12:15]\n\t"
"s_waitcnt vmcnt(0)\n\t"
"s_waitcnt lgkmcnt(0)\n\t"
"s_barrier\n\t"
"v_mfma_f32_16x16x32_bf16 v[44:47], v[188:191], v[136:139], v[44:47]\n\t"
"ds_read_b128 v[160:163], v250 offset:32768\n\t"
"v_mfma_f32_16x16x32_bf16 v[60:63], v[188:191], v[140:143], v[60:63]\n\t"
"ds_read_b128 v[164:167], v250 offset:34816\n\t"
"v_mfma_f32_16x16x32_bf16 v[56:59], v[184:187], v[140:143], v[56:59]\n\t"
"ds_read_b128 v[168:171], v250 offset:40960\n\t"
"v_mfma_f32_16x16x32_bf16 v[40:43], v[184:187], v[136:139], v[40:43]\n\t"
"ds_read_b128 v[172:175], v250 offset:43008\n\t"
"v_mfma_f32_16x16x32_bf16 v[36:39], v[180:183], v[136:139], v[36:39]\n\t"
"ds_read_b128 v[128:131], v248 offset:0\n\t"
"v_mfma_f32_16x16x32_bf16 v[52:55], v[180:183], v[140:143], v[52:55]\n\t"
"ds_read_b128 v[132:135], v248 offset:2048\n\t"
"v_mfma_f32_16x16x32_bf16 v[48:51], v[176:179], v[140:143], v[48:51]\n\t"
"v_mfma_f32_16x16x32_bf16 v[32:35], v[176:179], v[136:139], v[32:35]\n\t"
"v_mfma_f32_16x16x32_bf16 v[64:67], v[176:179], v[144:147], v[64:67]\n\t"
"ds_read_b128 v[136:139], v248 offset:4096\n\t"
"v_mfma_f32_16x16x32_bf16 v[80:83], v[176:179], v[148:151], v[80:83]\n\t"
"ds_read_b128 v[140:143], v248 offset:6144\n\t"
"v_mfma_f32_16x16x32_bf16 v[84:87], v[180:183], v[148:151], v[84:87]\n\t"
"v_mfma_f32_16x16x32_bf16 v[68:71], v[180:183], v[144:147], v[68:71]\n\t"
"v_mfma_f32_16x16x32_bf16 v[72:75], v[184:187], v[144:147], v[72:75]\n\t"
"v_mfma_f32_16x16x32_bf16 v[88:91], v[184:187], v[148:151], v[88:91]\n\t"
"v_mfma_f32_16x16x32_bf16 v[92:95], v[188:191], v[148:151], v[92:95]\n\t"
"v_mfma_f32_16x16x32_bf16 v[76:79], v[188:191], v[144:147], v[76:79]\n\t"
"v_mfma_f32_16x16x32_bf16 v[108:111], v[188:191], v[152:155], v[108:111]\n\t"
"ds_read_b128 v[144:147], v248 offset:16384\n\t"
"v_mfma_f32_16x16x32_bf16 v[124:127], v[188:191], v[156:159], v[124:127]\n\t"
"ds_read_b128 v[148:151], v248 offset:18432\n\t"
"v_mfma_f32_16x16x32_bf16 v[120:123], v[184:187], v[156:159], v[120:123]\n\t"
"v_mfma_f32_16x16x32_bf16 v[104:107], v[184:187], v[152:155], v[104:107]\n\t"
"v_mfma_f32_16x16x32_bf16 v[100:103], v[180:183], v[152:155], v[100:103]\n\t"
"v_mfma_f32_16x16x32_bf16 v[116:119], v[180:183], v[156:159], v[116:119]\n\t"
"v_mfma_f32_16x16x32_bf16 v[112:115], v[176:179], v[156:159], v[112:115]\n\t"
"v_mfma_f32_16x16x32_bf16 v[96:99], v[176:179], v[152:155], v[96:99]\n\t"
"ds_read_b128 v[152:155], v248 offset:20480\n\t"
"s_waitcnt lgkmcnt(3)\n\t"
"v_mfma_f32_16x16x32_bf16 v[0:3], v[160:163], v[128:131], v[0:3]\n\t"
"ds_read_b128 v[156:159], v248 offset:22528\n\t"
"v_mfma_f32_16x16x32_bf16 v[16:19], v[160:163], v[132:135], v[16:19]\n\t"
"ds_read_b128 v[176:179], v250 offset:33792\n\t"
"v_mfma_f32_16x16x32_bf16 v[20:23], v[164:167], v[132:135], v[20:23]\n\t"
"ds_read_b128 v[180:183], v250 offset:35840\n\t"
"v_mfma_f32_16x16x32_bf16 v[4:7], v[164:167], v[128:131], v[4:7]\n\t"
"ds_read_b128 v[184:187], v250 offset:41984\n\t"
"v_mfma_f32_16x16x32_bf16 v[8:11], v[168:171], v[128:131], v[8:11]\n\t"
"ds_read_b128 v[188:191], v250 offset:44032\n\t"
"v_mfma_f32_16x16x32_bf16 v[24:27], v[168:171], v[132:135], v[24:27]\n\t"
"v_mfma_f32_16x16x32_bf16 v[28:31], v[172:175], v[132:135], v[28:31]\n\t"
"v_mfma_f32_16x16x32_bf16 v[12:15], v[172:175], v[128:131], v[12:15]\n\t"
"v_mfma_f32_16x16x32_bf16 v[44:47], v[172:175], v[136:139], v[44:47]\n\t"
"ds_read_b128 v[128:131], v249 offset:0\n\t"
"v_mfma_f32_16x16x32_bf16 v[60:63], v[172:175], v[140:143], v[60:63]\n\t"
"ds_read_b128 v[132:135], v249 offset:2048\n\t"
"v_mfma_f32_16x16x32_bf16 v[56:59], v[168:171], v[140:143], v[56:59]\n\t"
"v_mfma_f32_16x16x32_bf16 v[40:43], v[168:171], v[136:139], v[40:43]\n\t"
"v_mfma_f32_16x16x32_bf16 v[36:39], v[164:167], v[136:139], v[36:39]\n\t"
"v_mfma_f32_16x16x32_bf16 v[52:55], v[164:167], v[140:143], v[52:55]\n\t"
"v_mfma_f32_16x16x32_bf16 v[48:51], v[160:163], v[140:143], v[48:51]\n\t"
"v_mfma_f32_16x16x32_bf16 v[32:35], v[160:163], v[136:139], v[32:35]\n\t"
"s_waitcnt lgkmcnt(2)\n\t"
"v_mfma_f32_16x16x32_bf16 v[64:67], v[160:163], v[144:147], v[64:67]\n\t"
"ds_read_b128 v[136:139], v249 offset:4096\n\t"
"v_mfma_f32_16x16x32_bf16 v[80:83], v[160:163], v[148:151], v[80:83]\n\t"
"ds_read_b128 v[140:143], v249 offset:6144\n\t"
"v_mfma_f32_16x16x32_bf16 v[84:87], v[164:167], v[148:151], v[84:87]\n\t"
"v_mfma_f32_16x16x32_bf16 v[68:71], v[164:167], v[144:147], v[68:71]\n\t"
"v_mfma_f32_16x16x32_bf16 v[72:75], v[168:171], v[144:147], v[72:75]\n\t"
"v_mfma_f32_16x16x32_bf16 v[88:91], v[168:171], v[148:151], v[88:91]\n\t"
"v_mfma_f32_16x16x32_bf16 v[92:95], v[172:175], v[148:151], v[92:95]\n\t"
"v_mfma_f32_16x16x32_bf16 v[76:79], v[172:175], v[144:147], v[76:79]\n\t"
"v_mfma_f32_16x16x32_bf16 v[108:111], v[172:175], v[152:155], v[108:111]\n\t"
"ds_read_b128 v[144:147], v249 offset:16384\n\t"
"v_mfma_f32_16x16x32_bf16 v[124:127], v[172:175], v[156:159], v[124:127]\n\t"
"ds_read_b128 v[148:151], v249 offset:18432\n\t"
"v_mfma_f32_16x16x32_bf16 v[120:123], v[168:171], v[156:159], v[120:123]\n\t"
"v_mfma_f32_16x16x32_bf16 v[104:107], v[168:171], v[152:155], v[104:107]\n\t"
"v_mfma_f32_16x16x32_bf16 v[100:103], v[164:167], v[152:155], v[100:103]\n\t"
"v_mfma_f32_16x16x32_bf16 v[116:119], v[164:167], v[156:159], v[116:119]\n\t"
"v_mfma_f32_16x16x32_bf16 v[112:115], v[160:163], v[156:159], v[112:115]\n\t"
"v_mfma_f32_16x16x32_bf16 v[96:99], v[160:163], v[152:155], v[96:99]\n\t"
"ds_read_b128 v[152:155], v249 offset:20480\n\t"
"s_waitcnt lgkmcnt(3)\n\t"
"v_mfma_f32_16x16x32_bf16 v[0:3], v[176:179], v[128:131], v[0:3]\n\t"
"ds_read_b128 v[156:159], v249 offset:22528\n\t"
"v_mfma_f32_16x16x32_bf16 v[16:19], v[176:179], v[132:135], v[16:19]\n\t"
"v_mfma_f32_16x16x32_bf16 v[20:23], v[180:183], v[132:135], v[20:23]\n\t"
"v_mfma_f32_16x16x32_bf16 v[4:7], v[180:183], v[128:131], v[4:7]\n\t"
"v_mfma_f32_16x16x32_bf16 v[8:11], v[184:187], v[128:131], v[8:11]\n\t"
"v_mfma_f32_16x16x32_bf16 v[24:27], v[184:187], v[132:135], v[24:27]\n\t"
"v_mfma_f32_16x16x32_bf16 v[28:31], v[188:191], v[132:135], v[28:31]\n\t"
"v_mfma_f32_16x16x32_bf16 v[12:15], v[188:191], v[128:131], v[12:15]\n\t"
"s_waitcnt lgkmcnt(0)\n\t"
"s_barrier\n\t"
"v_mfma_f32_16x16x32_bf16 v[44:47], v[188:191], v[136:139], v[44:47]\n\t"
"ds_read_b128 v[160:163], v250 offset:49152\n\t"
"v_mfma_f32_16x16x32_bf16 v[60:63], v[188:191], v[140:143], v[60:63]\n\t"
"ds_read_b128 v[164:167], v250 offset:51200\n\t"
"v_mfma_f32_16x16x32_bf16 v[56:59], v[184:187], v[140:143], v[56:59]\n\t"
"ds_read_b128 v[168:171], v250 offset:57344\n\t"
"v_mfma_f32_16x16x32_bf16 v[40:43], v[184:187], v[136:139], v[40:43]\n\t"
"ds_read_b128 v[172:175], v250 offset:59392\n\t"
"v_mfma_f32_16x16x32_bf16 v[36:39], v[180:183], v[136:139], v[36:39]\n\t"
"ds_read_b128 v[128:131], v248 offset:32768\n\t"
"v_mfma_f32_16x16x32_bf16 v[52:55], v[180:183], v[140:143], v[52:55]\n\t"
"ds_read_b128 v[132:135], v248 offset:34816\n\t"
"v_mfma_f32_16x16x32_bf16 v[48:51], v[176:179], v[140:143], v[48:51]\n\t"
"v_mfma_f32_16x16x32_bf16 v[32:35], v[176:179], v[136:139], v[32:35]\n\t"
"v_mfma_f32_16x16x32_bf16 v[64:67], v[176:179], v[144:147], v[64:67]\n\t"
"ds_read_b128 v[136:139], v248 offset:36864\n\t"
"v_mfma_f32_16x16x32_bf16 v[80:83], v[176:179], v[148:151], v[80:83]\n\t"
"ds_read_b128 v[140:143], v248 offset:38912\n\t"
"v_mfma_f32_16x16x32_bf16 v[84:87], v[180:183], v[148:151], v[84:87]\n\t"
"v_mfma_f32_16x16x32_bf16 v[68:71], v[180:183], v[144:147], v[68:71]\n\t"
"v_mfma_f32_16x16x32_bf16 v[72:75], v[184:187], v[144:147], v[72:75]\n\t"
"v_mfma_f32_16x16x32_bf16 v[88:91], v[184:187], v[148:151], v[88:91]\n\t"
"v_mfma_f32_16x16x32_bf16 v[92:95], v[188:191], v[148:151], v[92:95]\n\t"
"v_mfma_f32_16x16x32_bf16 v[76:79], v[188:191], v[144:147], v[76:79]\n\t"
"v_mfma_f32_16x16x32_bf16 v[108:111], v[188:191], v[152:155], v[108:111]\n\t"
"ds_read_b128 v[144:147], v248 offset:49152\n\t"
"v_mfma_f32_16x16x32_bf16 v[124:127], v[188:191], v[156:159], v[124:127]\n\t"
"ds_read_b128 v[148:151], v248 offset:51200\n\t"
"v_mfma_f32_16x16x32_bf16 v[120:123], v[184:187], v[156:159], v[120:123]\n\t"
"v_mfma_f32_16x16x32_bf16 v[104:107], v[184:187], v[152:155], v[104:107]\n\t"
"v_mfma_f32_16x16x32_bf16 v[100:103], v[180:183], v[152:155], v[100:103]\n\t"
"s_sub_u32 s56, s56, 1\n\t"
"v_mfma_f32_16x16x32_bf16 v[116:119], v[180:183], v[156:159], v[116:119]\n\t"
"s_cmp_lg_u32 s56, 0\n\t"
"v_mfma_f32_16x16x32_bf16 v[112:115], v[176:179], v[156:159], v[112:115]\n\t"
"v_mfma_f32_16x16x32_bf16 v[96:99], v[176:179], v[152:155], v[96:99]\n\t"
"ds_read_b128 v[152:155], v248 offset:53248\n\t"
"s_cbranch_scc1 L_g2c_%=\n\t"
"s_waitcnt lgkmcnt(3)\n\t"
"v_mfma_f32_16x16x32_bf16 v[0:3], v[160:163], v[128:131], v[0:3]\n\t"
"ds_read_b128 v[156:159], v248 offset:55296\n\t"
"v_mfma_f32_16x16x32_bf16 v[16:19], v[160:163], v[132:135], v[16:19]\n\t"
"ds_read_b128 v[176:179], v250 offset:50176\n\t"
"v_mfma_f32_16x16x32_bf16 v[20:23], v[164:167], v[132:135], v[20:23]\n\t"
"ds_read_b128 v[180:183], v250 offset:52224\n\t"
"v_mfma_f32_16x16x32_bf16 v[4:7], v[164:167], v[128:131], v[4:7]\n\t"
"ds_read_b128 v[184:187], v250 offset:58368\n\t"
"v_mfma_f32_16x16x32_bf16 v[8:11], v[168:171], v[128:131], v[8:11]\n\t"
"ds_read_b128 v[188:191], v250 offset:60416\n\t"
"v_mfma_f32_16x16x32_bf16 v[24:27], v[168:171], v[132:135], v[24:27]\n\t"
"v_mfma_f32_16x16x32_bf16 v[28:31], v[172:175], v[132:135], v[28:31]\n\t"
"v_mfma_f32_16x16x32_bf16 v[12:15], v[172:175], v[128:131], v[12:15]\n\t"
"v_mfma_f32_16x16x32_bf16 v[44:47], v[172:175], v[136:139], v[44:47]\n\t"
"ds_read_b128 v[128:131], v249 offset:32768\n\t"
"v_mfma_f32_16x16x32_bf16 v[60:63], v[172:175], v[140:143], v[60:63]\n\t"
"ds_read_b128 v[132:135], v249 offset:34816\n\t"
"v_mfma_f32_16x16x32_bf16 v[56:59], v[168:171], v[140:143], v[56:59]\n\t"
"v_mfma_f32_16x16x32_bf16 v[40:43], v[168:171], v[136:139], v[40:43]\n\t"
"v_mfma_f32_16x16x32_bf16 v[36:39], v[164:167], v[136:139], v[36:39]\n\t"
"v_mfma_f32_16x16x32_bf16 v[52:55], v[164:167], v[140:143], v[52:55]\n\t"
"v_mfma_f32_16x16x32_bf16 v[48:51], v[160:163], v[140:143], v[48:51]\n\t"
"v_mfma_f32_16x16x32_bf16 v[32:35], v[160:163], v[136:139], v[32:35]\n\t"
"s_waitcnt lgkmcnt(2)\n\t"
"v_mfma_f32_16x16x32_bf16 v[64:67], v[160:163], v[144:147], v[64:67]\n\t"
"ds_read_b128 v[136:139], v249 offset:36864\n\t"
"v_mfma_f32_16x16x32_bf16 v[80:83], v[160:163], v[148:151], v[80:83]\n\t"
"ds_read_b128 v[140:143], v249 offset:38912\n\t"
"v_mfma_f32_16x16x32_bf16 v[84:87], v[164:167], v[148:151], v[84:87]\n\t"
"v_mfma_f32_16x16x32_bf16 v[68:71], v[164:167], v[144:147], v[68:71]\n\t"
"v_mfma_f32_16x16x32_bf16 v[72:75], v[168:171], v[144:147], v[72:75]\n\t"
"v_mfma_f32_16x16x32_bf16 v[88:91], v[168:171], v[148:151], v[88:91]\n\t"
"v_mfma_f32_16x16x32_bf16 v[92:95], v[172:175], v[148:151], v[92:95]\n\t"
"v_mfma_f32_16x16x32_bf16 v[76:79], v[172:175], v[144:147], v[76:79]\n\t"
"v_mfma_f32_16x16x32_bf16 v[108:111], v[172:175], v[152:155], v[108:111]\n\t"
"ds_read_b128 v[144:147], v249 offset:49152\n\t"
"v_mfma_f32_16x16x32_bf16 v[124:127], v[172:175], v[156:159], v[124:127]\n\t"
"ds_read_b128 v[148:151], v249 offset:51200\n\t"
"v_mfma_f32_16x16x32_bf16 v[120:123], v[168:171], v[156:159], v[120:123]\n\t"
"v_mfma_f32_16x16x32_bf16 v[104:107], v[168:171], v[152:155], v[104:107]\n\t"
"v_mfma_f32_16x16x32_bf16 v[100:103], v[164:167], v[152:155], v[100:103]\n\t"
"v_mfma_f32_16x16x32_bf16 v[116:119], v[164:167], v[156:159], v[116:119]\n\t"
"v_mfma_f32_16x16x32_bf16 v[112:115], v[160:163], v[156:159], v[112:115]\n\t"
"v_mfma_f32_16x16x32_bf16 v[96:99], v[160:163], v[152:155], v[96:99]\n\t"
"ds_read_b128 v[152:155], v249 offset:53248\n\t"
"s_waitcnt lgkmcnt(3)\n\t"
"v_mfma_f32_16x16x32_bf16 v[0:3], v[176:179], v[128:131], v[0:3]\n\t"
"ds_read_b128 v[156:159], v249 offset:55296\n\t"
"v_mfma_f32_16x16x32_bf16 v[16:19], v[176:179], v[132:135], v[16:19]\n\t"
"v_mfma_f32_16x16x32_bf16 v[20:23], v[180:183], v[132:135], v[20:23]\n\t"
"v_mfma_f32_16x16x32_bf16 v[4:7], v[180:183], v[128:131], v[4:7]\n\t"
"v_mfma_f32_16x16x32_bf16 v[8:11], v[184:187], v[128:131], v[8:11]\n\t"
"v_mfma_f32_16x16x32_bf16 v[24:27], v[184:187], v[132:135], v[24:27]\n\t"
"v_mfma_f32_16x16x32_bf16 v[28:31], v[188:191], v[132:135], v[28:31]\n\t"
"v_mfma_f32_16x16x32_bf16 v[12:15], v[188:191], v[128:131], v[12:15]\n\t"
"s_waitcnt lgkmcnt(0)\n\t"
"s_barrier\n\t"
"v_mfma_f32_16x16x32_bf16 v[44:47], v[188:191], v[136:139], v[44:47]\n\t"
"ds_read_b128 v[160:163], v250 offset:0\n\t"
"v_mfma_f32_16x16x32_bf16 v[60:63], v[188:191], v[140:143], v[60:63]\n\t"
"ds_read_b128 v[164:167], v250 offset:2048\n\t"
"v_mfma_f32_16x16x32_bf16 v[56:59], v[184:187], v[140:143], v[56:59]\n\t"
"ds_read_b128 v[168:171], v250 offset:8192\n\t"
"v_mfma_f32_16x16x32_bf16 v[40:43], v[184:187], v[136:139], v[40:43]\n\t"
"ds_read_b128 v[172:175], v250 offset:10240\n\t"
"v_mfma_f32_16x16x32_bf16 v[36:39], v[180:183], v[136:139], v[36:39]\n\t"
"ds_read_b128 v[128:131], v228 offset:0\n\t"
"v_mfma_f32_16x16x32_bf16 v[52:55], v[180:183], v[140:143], v[52:55]\n\t"
"ds_read_b128 v[132:135], v228 offset:2048\n\t"
"v_mfma_f32_16x16x32_bf16 v[48:51], v[176:179], v[140:143], v[48:51]\n\t"
"v_mfma_f32_16x16x32_bf16 v[32:35], v[176:179], v[136:139], v[32:35]\n\t"
"v_mfma_f32_16x16x32_bf16 v[64:67], v[176:179], v[144:147], v[64:67]\n\t"
"ds_read_b128 v[136:139], v228 offset:4096\n\t"
"v_mfma_f32_16x16x32_bf16 v[80:83], v[176:179], v[148:151], v[80:83]\n\t"
"ds_read_b128 v[140:143], v228 offset:6144\n\t"
"v_mfma_f32_16x16x32_bf16 v[84:87], v[180:183], v[148:151], v[84:87]\n\t"
"v_mfma_f32_16x16x32_bf16 v[68:71], v[180:183], v[144:147], v[68:71]\n\t"
"v_mfma_f32_16x16x32_bf16 v[72:75], v[184:187], v[144:147], v[72:75]\n\t"
"v_mfma_f32_16x16x32_bf16 v[88:91], v[184:187], v[148:151], v[88:91]\n\t"
"v_mfma_f32_16x16x32_bf16 v[92:95], v[188:191], v[148:151], v[92:95]\n\t"
"v_mfma_f32_16x16x32_bf16 v[76:79], v[188:191], v[144:147], v[76:79]\n\t"
"v_mfma_f32_16x16x32_bf16 v[108:111], v[188:191], v[152:155], v[108:111]\n\t"
"ds_read_b128 v[144:147], v228 offset:16384\n\t"
"v_mfma_f32_16x16x32_bf16 v[124:127], v[188:191], v[156:159], v[124:127]\n\t"
"ds_read_b128 v[148:151], v228 offset:18432\n\t"
"v_mfma_f32_16x16x32_bf16 v[120:123], v[184:187], v[156:159], v[120:123]\n\t"
"v_mfma_f32_16x16x32_bf16 v[104:107], v[184:187], v[152:155], v[104:107]\n\t"
"v_mfma_f32_16x16x32_bf16 v[100:103], v[180:183], v[152:155], v[100:103]\n\t"
"v_mfma_f32_16x16x32_bf16 v[116:119], v[180:183], v[156:159], v[116:119]\n\t"
"v_mfma_f32_16x16x32_bf16 v[112:115], v[176:179], v[156:159], v[112:115]\n\t"
"v_mfma_f32_16x16x32_bf16 v[96:99], v[176:179], v[152:155], v[96:99]\n\t"
"ds_read_b128 v[152:155], v228 offset:20480\n\t"
"s_waitcnt lgkmcnt(3)\n\t"
"v_mfma_f32_16x16x32_bf16 v[0:3], v[160:163], v[128:131], v[0:3]\n\t"
"ds_read_b128 v[156:159], v228 offset:22528\n\t"
"v_mfma_f32_16x16x32_bf16 v[16:19], v[160:163], v[132:135], v[16:19]\n\t"
"ds_read_b128 v[176:179], v250 offset:1024\n\t"
"v_mfma_f32_16x16x32_bf16 v[20:23], v[164:167], v[132:135], v[20:23]\n\t"
"ds_read_b128 v[180:183], v250 offset:3072\n\t"
"v_mfma_f32_16x16x32_bf16 v[4:7], v[164:167], v[128:131], v[4:7]\n\t"
"ds_read_b128 v[184:187], v250 offset:9216\n\t"
"v_mfma_f32_16x16x32_bf16 v[8:11], v[168:171], v[128:131], v[8:11]\n\t"
"ds_read_b128 v[188:191], v250 offset:11264\n\t"
"v_mfma_f32_16x16x32_bf16 v[24:27], v[168:171], v[132:135], v[24:27]\n\t"
"v_mfma_f32_16x16x32_bf16 v[28:31], v[172:175], v[132:135], v[28:31]\n\t"
"v_mfma_f32_16x16x32_bf16 v[12:15], v[172:175], v[128:131], v[12:15]\n\t"
"v_mfma_f32_16x16x32_bf16 v[44:47], v[172:175], v[136:139], v[44:47]\n\t"
"ds_read_b128 v[128:131], v229 offset:0\n\t"
"v_mfma_f32_16x16x32_bf16 v[60:63], v[172:175], v[140:143], v[60:63]\n\t"
"ds_read_b128 v[132:135], v229 offset:2048\n\t"
"v_mfma_f32_16x16x32_bf16 v[56:59], v[168:171], v[140:143], v[56:59]\n\t"
"v_mfma_f32_16x16x32_bf16 v[40:43], v[168:171], v[136:139], v[40:43]\n\t"
"v_mfma_f32_16x16x32_bf16 v[36:39], v[164:167], v[136:139], v[36:39]\n\t"
"v_mfma_f32_16x16x32_bf16 v[52:55], v[164:167], v[140:143], v[52:55]\n\t"
"v_mfma_f32_16x16x32_bf16 v[48:51], v[160:163], v[140:143], v[48:51]\n\t"
"v_mfma_f32_16x16x32_bf16 v[32:35], v[160:163], v[136:139], v[32:35]\n\t"
"s_waitcnt lgkmcnt(2)\n\t"
"v_mfma_f32_16x16x32_bf16 v[64:67], v[160:163], v[144:147], v[64:67]\n\t"
"ds_read_b128 v[136:139], v229 offset:4096\n\t"
"v_mfma_f32_16x16x32_bf16 v[80:83], v[160:163], v[148:151], v[80:83]\n\t"
"ds_read_b128 v[140:143], v229 offset:6144\n\t"
"v_mfma_f32_16x16x32_bf16 v[84:87], v[164:167], v[148:151], v[84:87]\n\t"
"v_mfma_f32_16x16x32_bf16 v[68:71], v[164:167], v[144:147], v[68:71]\n\t"
"v_mfma_f32_16x16x32_bf16 v[72:75], v[168:171], v[144:147], v[72:75]\n\t"
"v_mfma_f32_16x16x32_bf16 v[88:91], v[168:171], v[148:151], v[88:91]\n\t"
"v_mfma_f32_16x16x32_bf16 v[92:95], v[172:175], v[148:151], v[92:95]\n\t"
"v_mfma_f32_16x16x32_bf16 v[76:79], v[172:175], v[144:147], v[76:79]\n\t"
"v_mfma_f32_16x16x32_bf16 v[108:111], v[172:175], v[152:155], v[108:111]\n\t"
"ds_read_b128 v[144:147], v229 offset:16384\n\t"
"v_mfma_f32_16x16x32_bf16 v[124:127], v[172:175], v[156:159], v[124:127]\n\t"
"ds_read_b128 v[148:151], v229 offset:18432\n\t"
"v_mfma_f32_16x16x32_bf16 v[120:123], v[168:171], v[156:159], v[120:123]\n\t"
"v_mfma_f32_16x16x32_bf16 v[104:107], v[168:171], v[152:155], v[104:107]\n\t"
"v_mfma_f32_16x16x32_bf16 v[100:103], v[164:167], v[152:155], v[100:103]\n\t"
"v_mfma_f32_16x16x32_bf16 v[116:119], v[164:167], v[156:159], v[116:119]\n\t"
"v_mfma_f32_16x16x32_bf16 v[112:115], v[160:163], v[156:159], v[112:115]\n\t"
"v_mfma_f32_16x16x32_bf16 v[96:99], v[160:163], v[152:155], v[96:99]\n\t"
"ds_read_b128 v[152:155], v229 offset:20480\n\t"
"s_waitcnt lgkmcnt(3)\n\t"
"v_mfma_f32_16x16x32_bf16 v[0:3], v[176:179], v[128:131], v[0:3]\n\t"
"ds_read_b128 v[156:159], v229 offset:22528\n\t"
"v_mfma_f32_16x16x32_bf16 v[16:19], v[176:179], v[132:135], v[16:19]\n\t"
"v_mfma_f32_16x16x32_bf16 v[20:23], v[180:183], v[132:135], v[20:23]\n\t"
"v_mfma_f32_16x16x32_bf16 v[4:7], v[180:183], v[128:131], v[4:7]\n\t"
"v_mfma_f32_16x16x32_bf16 v[8:11], v[184:187], v[128:131], v[8:11]\n\t"
"v_mfma_f32_16x16x32_bf16 v[24:27], v[184:187], v[132:135], v[24:27]\n\t"
"v_mfma_f32_16x16x32_bf16 v[28:31], v[188:191], v[132:135], v[28:31]\n\t"
"v_mfma_f32_16x16x32_bf16 v[12:15], v[188:191], v[128:131], v[12:15]\n\t"
"s_waitcnt lgkmcnt(0)\n\t"
"s_barrier\n\t"
"v_mfma_f32_16x16x32_bf16 v[44:47], v[188:191], v[136:139], v[44:47]\n\t"
"ds_read_b128 v[160:163], v250 offset:32768\n\t"
"v_mfma_f32_16x16x32_bf16 v[60:63], v[188:191], v[140:143], v[60:63]\n\t"
"ds_read_b128 v[164:167], v250 offset:34816\n\t"
"v_mfma_f32_16x16x32_bf16 v[56:59], v[184:187], v[140:143], v[56:59]\n\t"
"ds_read_b128 v[168:171], v250 offset:40960\n\t"
"v_mfma_f32_16x16x32_bf16 v[40:43], v[184:187], v[136:139], v[40:43]\n\t"
"ds_read_b128 v[172:175], v250 offset:43008\n\t"
"v_mfma_f32_16x16x32_bf16 v[36:39], v[180:183], v[136:139], v[36:39]\n\t"
"ds_read_b128 v[128:131], v248 offset:0\n\t"
"v_mfma_f32_16x16x32_bf16 v[52:55], v[180:183], v[140:143], v[52:55]\n\t"
"ds_read_b128 v[132:135], v248 offset:2048\n\t"
"v_mfma_f32_16x16x32_bf16 v[48:51], v[176:179], v[140:143], v[48:51]\n\t"
"v_mfma_f32_16x16x32_bf16 v[32:35], v[176:179], v[136:139], v[32:35]\n\t"
"v_mfma_f32_16x16x32_bf16 v[64:67], v[176:179], v[144:147], v[64:67]\n\t"
"ds_read_b128 v[136:139], v248 offset:4096\n\t"
"v_mfma_f32_16x16x32_bf16 v[80:83], v[176:179], v[148:151], v[80:83]\n\t"
"ds_read_b128 v[140:143], v248 offset:6144\n\t"
"v_mfma_f32_16x16x32_bf16 v[84:87], v[180:183], v[148:151], v[84:87]\n\t"
"v_mfma_f32_16x16x32_bf16 v[68:71], v[180:183], v[144:147], v[68:71]\n\t"
"v_mfma_f32_16x16x32_bf16 v[72:75], v[184:187], v[144:147], v[72:75]\n\t"
"v_mfma_f32_16x16x32_bf16 v[88:91], v[184:187], v[148:151], v[88:91]\n\t"
"v_mfma_f32_16x16x32_bf16 v[92:95], v[188:191], v[148:151], v[92:95]\n\t"
"v_mfma_f32_16x16x32_bf16 v[76:79], v[188:191], v[144:147], v[76:79]\n\t"
"v_mfma_f32_16x16x32_bf16 v[108:111], v[188:191], v[152:155], v[108:111]\n\t"
"ds_read_b128 v[144:147], v248 offset:16384\n\t"
"v_mfma_f32_16x16x32_bf16 v[124:127], v[188:191], v[156:159], v[124:127]\n\t"
"ds_read_b128 v[148:151], v248 offset:18432\n\t"
"v_mfma_f32_16x16x32_bf16 v[120:123], v[184:187], v[156:159], v[120:123]\n\t"
"v_mfma_f32_16x16x32_bf16 v[104:107], v[184:187], v[152:155], v[104:107]\n\t"
"v_mfma_f32_16x16x32_bf16 v[100:103], v[180:183], v[152:155], v[100:103]\n\t"
"v_mfma_f32_16x16x32_bf16 v[116:119], v[180:183], v[156:159], v[116:119]\n\t"
"v_mfma_f32_16x16x32_bf16 v[112:115], v[176:179], v[156:159], v[112:115]\n\t"
"v_mfma_f32_16x16x32_bf16 v[96:99], v[176:179], v[152:155], v[96:99]\n\t"
"ds_read_b128 v[152:155], v248 offset:20480\n\t"
"s_waitcnt lgkmcnt(3)\n\t"
"v_mfma_f32_16x16x32_bf16 v[0:3], v[160:163], v[128:131], v[0:3]\n\t"
"ds_read_b128 v[156:159], v248 offset:22528\n\t"
"v_mfma_f32_16x16x32_bf16 v[16:19], v[160:163], v[132:135], v[16:19]\n\t"
"ds_read_b128 v[176:179], v250 offset:33792\n\t"
"v_mfma_f32_16x16x32_bf16 v[20:23], v[164:167], v[132:135], v[20:23]\n\t"
"ds_read_b128 v[180:183], v250 offset:35840\n\t"
"v_mfma_f32_16x16x32_bf16 v[4:7], v[164:167], v[128:131], v[4:7]\n\t"
"ds_read_b128 v[184:187], v250 offset:41984\n\t"
"v_mfma_f32_16x16x32_bf16 v[8:11], v[168:171], v[128:131], v[8:11]\n\t"
"ds_read_b128 v[188:191], v250 offset:44032\n\t"
"v_mfma_f32_16x16x32_bf16 v[24:27], v[168:171], v[132:135], v[24:27]\n\t"
"v_mfma_f32_16x16x32_bf16 v[28:31], v[172:175], v[132:135], v[28:31]\n\t"
"v_mfma_f32_16x16x32_bf16 v[12:15], v[172:175], v[128:131], v[12:15]\n\t"
"v_mfma_f32_16x16x32_bf16 v[44:47], v[172:175], v[136:139], v[44:47]\n\t"
"ds_read_b128 v[128:131], v249 offset:0\n\t"
"v_mfma_f32_16x16x32_bf16 v[60:63], v[172:175], v[140:143], v[60:63]\n\t"
"ds_read_b128 v[132:135], v249 offset:2048\n\t"
"v_mfma_f32_16x16x32_bf16 v[56:59], v[168:171], v[140:143], v[56:59]\n\t"
"v_mfma_f32_16x16x32_bf16 v[40:43], v[168:171], v[136:139], v[40:43]\n\t"
"v_mfma_f32_16x16x32_bf16 v[36:39], v[164:167], v[136:139], v[36:39]\n\t"
"v_mfma_f32_16x16x32_bf16 v[52:55], v[164:167], v[140:143], v[52:55]\n\t"
"v_mfma_f32_16x16x32_bf16 v[48:51], v[160:163], v[140:143], v[48:51]\n\t"
"v_mfma_f32_16x16x32_bf16 v[32:35], v[160:163], v[136:139], v[32:35]\n\t"
"s_waitcnt lgkmcnt(2)\n\t"
"v_mfma_f32_16x16x32_bf16 v[64:67], v[160:163], v[144:147], v[64:67]\n\t"
"ds_read_b128 v[136:139], v249 offset:4096\n\t"
"v_mfma_f32_16x16x32_bf16 v[80:83], v[160:163], v[148:151], v[80:83]\n\t"
"ds_read_b128 v[140:143], v249 offset:6144\n\t"
"v_mfma_f32_16x16x32_bf16 v[84:87], v[164:167], v[148:151], v[84:87]\n\t"
"v_mfma_f32_16x16x32_bf16 v[68:71], v[164:167], v[144:147], v[68:71]\n\t"
"v_mfma_f32_16x16x32_bf16 v[72:75], v[168:171], v[144:147], v[72:75]\n\t"
"v_mfma_f32_16x16x32_bf16 v[88:91], v[168:171], v[148:151], v[88:91]\n\t"
"v_mfma_f32_16x16x32_bf16 v[92:95], v[172:175], v[148:151], v[92:95]\n\t"
"v_mfma_f32_16x16x32_bf16 v[76:79], v[172:175], v[144:147], v[76:79]\n\t"
"v_mfma_f32_16x16x32_bf16 v[108:111], v[172:175], v[152:155], v[108:111]\n\t"
"ds_read_b128 v[144:147], v249 offset:16384\n\t"
"v_mfma_f32_16x16x32_bf16 v[124:127], v[172:175], v[156:159], v[124:127]\n\t"
"ds_read_b128 v[148:151], v249 offset:18432\n\t"
"v_mfma_f32_16x16x32_bf16 v[120:123], v[168:171], v[156:159], v[120:123]\n\t"
"v_mfma_f32_16x16x32_bf16 v[104:107], v[168:171], v[152:155], v[104:107]\n\t"
"v_mfma_f32_16x16x32_bf16 v[100:103], v[164:167], v[152:155], v[100:103]\n\t"
"v_mfma_f32_16x16x32_bf16 v[116:119], v[164:167], v[156:159], v[116:119]\n\t"
"v_mfma_f32_16x16x32_bf16 v[112:115], v[160:163], v[156:159], v[112:115]\n\t"
"v_mfma_f32_16x16x32_bf16 v[96:99], v[160:163], v[152:155], v[96:99]\n\t"
"ds_read_b128 v[152:155], v249 offset:20480\n\t"
"s_waitcnt lgkmcnt(3)\n\t"
"v_mfma_f32_16x16x32_bf16 v[0:3], v[176:179], v[128:131], v[0:3]\n\t"
"ds_read_b128 v[156:159], v249 offset:22528\n\t"
"v_mfma_f32_16x16x32_bf16 v[16:19], v[176:179], v[132:135], v[16:19]\n\t"
"v_mfma_f32_16x16x32_bf16 v[20:23], v[180:183], v[132:135], v[20:23]\n\t"
"v_mfma_f32_16x16x32_bf16 v[4:7], v[180:183], v[128:131], v[4:7]\n\t"
"v_mfma_f32_16x16x32_bf16 v[8:11], v[184:187], v[128:131], v[8:11]\n\t"
"v_mfma_f32_16x16x32_bf16 v[24:27], v[184:187], v[132:135], v[24:27]\n\t"
"v_mfma_f32_16x16x32_bf16 v[28:31], v[188:191], v[132:135], v[28:31]\n\t"
"v_mfma_f32_16x16x32_bf16 v[12:15], v[188:191], v[128:131], v[12:15]\n\t"
"s_waitcnt lgkmcnt(0)\n\t"
"s_barrier\n\t"
"v_mfma_f32_16x16x32_bf16 v[44:47], v[188:191], v[136:139], v[44:47]\n\t"
"v_add_u32 v248, s91, v238\n\t"
"v_mfma_f32_16x16x32_bf16 v[60:63], v[188:191], v[140:143], v[60:63]\n\t"
"v_add_u32 v228, 0x10000, v248\n\t"
"v_mfma_f32_16x16x32_bf16 v[56:59], v[184:187], v[140:143], v[56:59]\n\t"
"v_add_u32 v249, 1024, v248\n\t"
"v_mfma_f32_16x16x32_bf16 v[40:43], v[184:187], v[136:139], v[40:43]\n\t"
"v_add_u32 v229, 1024, v228\n\t"
"v_mfma_f32_16x16x32_bf16 v[36:39], v[180:183], v[136:139], v[36:39]\n\t"
"ds_read_b128 v[160:163], v250 offset:49152\n\t"
"v_mfma_f32_16x16x32_bf16 v[52:55], v[180:183], v[140:143], v[52:55]\n\t"
"ds_read_b128 v[164:167], v250 offset:51200\n\t"
"v_mfma_f32_16x16x32_bf16 v[48:51], v[176:179], v[140:143], v[48:51]\n\t"
"ds_read_b128 v[168:171], v250 offset:57344\n\t"
"v_mfma_f32_16x16x32_bf16 v[32:35], v[176:179], v[136:139], v[32:35]\n\t"
"ds_read_b128 v[172:175], v250 offset:59392\n\t"
"v_mfma_f32_16x16x32_bf16 v[64:67], v[176:179], v[144:147], v[64:67]\n\t"
"ds_read_b128 v[128:131], v248 offset:32768\n\t"
"v_mfma_f32_16x16x32_bf16 v[80:83], v[176:179], v[148:151], v[80:83]\n\t"
"ds_read_b128 v[132:135], v248 offset:34816\n\t"
"v_mfma_f32_16x16x32_bf16 v[84:87], v[180:183], v[148:151], v[84:87]\n\t"
"ds_read_b128 v[136:139], v248 offset:36864\n\t"
"v_mfma_f32_16x16x32_bf16 v[68:71], v[180:183], v[144:147], v[68:71]\n\t"
"ds_read_b128 v[140:143], v248 offset:38912\n\t"
"v_mfma_f32_16x16x32_bf16 v[72:75], v[184:187], v[144:147], v[72:75]\n\t"
"v_mfma_f32_16x16x32_bf16 v[88:91], v[184:187], v[148:151], v[88:91]\n\t"
"v_mfma_f32_16x16x32_bf16 v[92:95], v[188:191], v[148:151], v[92:95]\n\t"
"v_mfma_f32_16x16x32_bf16 v[76:79], v[188:191], v[144:147], v[76:79]\n\t"
"v_mfma_f32_16x16x32_bf16 v[108:111], v[188:191], v[152:155], v[108:111]\n\t"
"ds_read_b128 v[144:147], v248 offset:49152\n\t"
"v_mfma_f32_16x16x32_bf16 v[124:127], v[188:191], v[156:159], v[124:127]\n\t"
"ds_read_b128 v[148:151], v248 offset:51200\n\t"
"v_mfma_f32_16x16x32_bf16 v[120:123], v[184:187], v[156:159], v[120:123]\n\t"
"v_mfma_f32_16x16x32_bf16 v[104:107], v[184:187], v[152:155], v[104:107]\n\t"
"v_mfma_f32_16x16x32_bf16 v[100:103], v[180:183], v[152:155], v[100:103]\n\t"
"v_mfma_f32_16x16x32_bf16 v[116:119], v[180:183], v[156:159], v[116:119]\n\t"
"v_mfma_f32_16x16x32_bf16 v[112:115], v[176:179], v[156:159], v[112:115]\n\t"
"v_mfma_f32_16x16x32_bf16 v[96:99], v[176:179], v[152:155], v[96:99]\n\t"
"ds_read_b128 v[152:155], v248 offset:53248\n\t"
"s_waitcnt lgkmcnt(3)\n\t"
"v_mfma_f32_16x16x32_bf16 v[0:3], v[160:163], v[128:131], v[0:3]\n\t"
"ds_read_b128 v[156:159], v248 offset:55296\n\t"
"v_mfma_f32_16x16x32_bf16 v[16:19], v[160:163], v[132:135], v[16:19]\n\t"
"ds_read_b128 v[176:179], v250 offset:50176\n\t"
"v_mfma_f32_16x16x32_bf16 v[20:23], v[164:167], v[132:135], v[20:23]\n\t"
"ds_read_b128 v[180:183], v250 offset:52224\n\t"
"v_mfma_f32_16x16x32_bf16 v[4:7], v[164:167], v[128:131], v[4:7]\n\t"
"ds_read_b128 v[184:187], v250 offset:58368\n\t"
"v_mfma_f32_16x16x32_bf16 v[8:11], v[168:171], v[128:131], v[8:11]\n\t"
"ds_read_b128 v[188:191], v250 offset:60416\n\t"
"v_mfma_f32_16x16x32_bf16 v[24:27], v[168:171], v[132:135], v[24:27]\n\t"
"v_mfma_f32_16x16x32_bf16 v[28:31], v[172:175], v[132:135], v[28:31]\n\t"
"v_mfma_f32_16x16x32_bf16 v[12:15], v[172:175], v[128:131], v[12:15]\n\t"
"v_mfma_f32_16x16x32_bf16 v[44:47], v[172:175], v[136:139], v[44:47]\n\t"
"ds_read_b128 v[128:131], v249 offset:32768\n\t"
"v_mfma_f32_16x16x32_bf16 v[60:63], v[172:175], v[140:143], v[60:63]\n\t"
"ds_read_b128 v[132:135], v249 offset:34816\n\t"
"v_mfma_f32_16x16x32_bf16 v[56:59], v[168:171], v[140:143], v[56:59]\n\t"
"v_mfma_f32_16x16x32_bf16 v[40:43], v[168:171], v[136:139], v[40:43]\n\t"
"v_mfma_f32_16x16x32_bf16 v[36:39], v[164:167], v[136:139], v[36:39]\n\t"
"v_mfma_f32_16x16x32_bf16 v[52:55], v[164:167], v[140:143], v[52:55]\n\t"
"v_mfma_f32_16x16x32_bf16 v[48:51], v[160:163], v[140:143], v[48:51]\n\t"
"v_mfma_f32_16x16x32_bf16 v[32:35], v[160:163], v[136:139], v[32:35]\n\t"
"s_waitcnt lgkmcnt(2)\n\t"
"v_mfma_f32_16x16x32_bf16 v[64:67], v[160:163], v[144:147], v[64:67]\n\t"
"ds_read_b128 v[136:139], v249 offset:36864\n\t"
"v_mfma_f32_16x16x32_bf16 v[80:83], v[160:163], v[148:151], v[80:83]\n\t"
"ds_read_b128 v[140:143], v249 offset:38912\n\t"
"v_mfma_f32_16x16x32_bf16 v[84:87], v[164:167], v[148:151], v[84:87]\n\t"
"v_mfma_f32_16x16x32_bf16 v[68:71], v[164:167], v[144:147], v[68:71]\n\t"
"v_mfma_f32_16x16x32_bf16 v[72:75], v[168:171], v[144:147], v[72:75]\n\t"
"v_mfma_f32_16x16x32_bf16 v[88:91], v[168:171], v[148:151], v[88:91]\n\t"
"v_mfma_f32_16x16x32_bf16 v[92:95], v[172:175], v[148:151], v[92:95]\n\t"
"v_mfma_f32_16x16x32_bf16 v[76:79], v[172:175], v[144:147], v[76:79]\n\t"
"v_mfma_f32_16x16x32_bf16 v[108:111], v[172:175], v[152:155], v[108:111]\n\t"
"ds_read_b128 v[144:147], v249 offset:49152\n\t"
"v_mfma_f32_16x16x32_bf16 v[124:127], v[172:175], v[156:159], v[124:127]\n\t"
"ds_read_b128 v[148:151], v249 offset:51200\n\t"
"v_mfma_f32_16x16x32_bf16 v[120:123], v[168:171], v[156:159], v[120:123]\n\t"
"v_mfma_f32_16x16x32_bf16 v[104:107], v[168:171], v[152:155], v[104:107]\n\t"
"v_mfma_f32_16x16x32_bf16 v[100:103], v[164:167], v[152:155], v[100:103]\n\t"
"v_mfma_f32_16x16x32_bf16 v[116:119], v[164:167], v[156:159], v[116:119]\n\t"
"v_mfma_f32_16x16x32_bf16 v[112:115], v[160:163], v[156:159], v[112:115]\n\t"
"v_mfma_f32_16x16x32_bf16 v[96:99], v[160:163], v[152:155], v[96:99]\n\t"
"ds_read_b128 v[152:155], v249 offset:53248\n\t"
"s_waitcnt lgkmcnt(3)\n\t"
"v_mfma_f32_16x16x32_bf16 v[0:3], v[176:179], v[128:131], v[0:3]\n\t"
"ds_read_b128 v[156:159], v249 offset:55296\n\t"
"v_mfma_f32_16x16x32_bf16 v[16:19], v[176:179], v[132:135], v[16:19]\n\t"
"v_mfma_f32_16x16x32_bf16 v[20:23], v[180:183], v[132:135], v[20:23]\n\t"
"v_mfma_f32_16x16x32_bf16 v[4:7], v[180:183], v[128:131], v[4:7]\n\t"
"v_mfma_f32_16x16x32_bf16 v[8:11], v[184:187], v[128:131], v[8:11]\n\t"
"v_mfma_f32_16x16x32_bf16 v[24:27], v[184:187], v[132:135], v[24:27]\n\t"
"v_mfma_f32_16x16x32_bf16 v[28:31], v[188:191], v[132:135], v[28:31]\n\t"
"v_mfma_f32_16x16x32_bf16 v[12:15], v[188:191], v[128:131], v[12:15]\n\t"
"s_waitcnt lgkmcnt(0)\n\t"
"s_barrier\n\t"
"v_mfma_f32_16x16x32_bf16 v[44:47], v[188:191], v[136:139], v[44:47]\n\t"
"ds_read_b128 v[160:163], v250 offset:0\n\t"
"v_mfma_f32_16x16x32_bf16 v[60:63], v[188:191], v[140:143], v[60:63]\n\t"
"ds_read_b128 v[164:167], v250 offset:2048\n\t"
"v_mfma_f32_16x16x32_bf16 v[56:59], v[184:187], v[140:143], v[56:59]\n\t"
"ds_read_b128 v[168:171], v250 offset:8192\n\t"
"v_mfma_f32_16x16x32_bf16 v[40:43], v[184:187], v[136:139], v[40:43]\n\t"
"ds_read_b128 v[172:175], v250 offset:10240\n\t"
"v_mfma_f32_16x16x32_bf16 v[36:39], v[180:183], v[136:139], v[36:39]\n\t"
"ds_read_b128 v[128:131], v228 offset:0\n\t"
"v_mfma_f32_16x16x32_bf16 v[52:55], v[180:183], v[140:143], v[52:55]\n\t"
"ds_read_b128 v[132:135], v228 offset:2048\n\t"
"v_mfma_f32_16x16x32_bf16 v[48:51], v[176:179], v[140:143], v[48:51]\n\t"
"v_mfma_f32_16x16x32_bf16 v[32:35], v[176:179], v[136:139], v[32:35]\n\t"
"v_mfma_f32_16x16x32_bf16 v[64:67], v[176:179], v[144:147], v[64:67]\n\t"
"ds_read_b128 v[136:139], v228 offset:4096\n\t"
"v_mfma_f32_16x16x32_bf16 v[80:83], v[176:179], v[148:151], v[80:83]\n\t"
"ds_read_b128 v[140:143], v228 offset:6144\n\t"
"v_mfma_f32_16x16x32_bf16 v[84:87], v[180:183], v[148:151], v[84:87]\n\t"
"v_mfma_f32_16x16x32_bf16 v[68:71], v[180:183], v[144:147], v[68:71]\n\t"
"v_mfma_f32_16x16x32_bf16 v[72:75], v[184:187], v[144:147], v[72:75]\n\t"
"v_mfma_f32_16x16x32_bf16 v[88:91], v[184:187], v[148:151], v[88:91]\n\t"
"v_mfma_f32_16x16x32_bf16 v[92:95], v[188:191], v[148:151], v[92:95]\n\t"
"v_mfma_f32_16x16x32_bf16 v[76:79], v[188:191], v[144:147], v[76:79]\n\t"
"v_mfma_f32_16x16x32_bf16 v[108:111], v[188:191], v[152:155], v[108:111]\n\t"
"ds_read_b128 v[144:147], v228 offset:16384\n\t"
"v_mfma_f32_16x16x32_bf16 v[124:127], v[188:191], v[156:159], v[124:127]\n\t"
"ds_read_b128 v[148:151], v228 offset:18432\n\t"
"v_mfma_f32_16x16x32_bf16 v[120:123], v[184:187], v[156:159], v[120:123]\n\t"
"v_mfma_f32_16x16x32_bf16 v[104:107], v[184:187], v[152:155], v[104:107]\n\t"
"v_mfma_f32_16x16x32_bf16 v[100:103], v[180:183], v[152:155], v[100:103]\n\t"
"v_mfma_f32_16x16x32_bf16 v[116:119], v[180:183], v[156:159], v[116:119]\n\t"
"v_mfma_f32_16x16x32_bf16 v[112:115], v[176:179], v[156:159], v[112:115]\n\t"
"v_mfma_f32_16x16x32_bf16 v[96:99], v[176:179], v[152:155], v[96:99]\n\t"
"ds_read_b128 v[152:155], v228 offset:20480\n\t"
"s_waitcnt lgkmcnt(3)\n\t"
"v_mfma_f32_16x16x32_bf16 v[0:3], v[160:163], v[128:131], v[0:3]\n\t"
"ds_read_b128 v[156:159], v228 offset:22528\n\t"
"v_mfma_f32_16x16x32_bf16 v[16:19], v[160:163], v[132:135], v[16:19]\n\t"
"ds_read_b128 v[176:179], v250 offset:1024\n\t"
"v_mfma_f32_16x16x32_bf16 v[20:23], v[164:167], v[132:135], v[20:23]\n\t"
"ds_read_b128 v[180:183], v250 offset:3072\n\t"
"v_mfma_f32_16x16x32_bf16 v[4:7], v[164:167], v[128:131], v[4:7]\n\t"
"ds_read_b128 v[184:187], v250 offset:9216\n\t"
"v_mfma_f32_16x16x32_bf16 v[8:11], v[168:171], v[128:131], v[8:11]\n\t"
"ds_read_b128 v[188:191], v250 offset:11264\n\t"
"v_mfma_f32_16x16x32_bf16 v[24:27], v[168:171], v[132:135], v[24:27]\n\t"
"v_mfma_f32_16x16x32_bf16 v[28:31], v[172:175], v[132:135], v[28:31]\n\t"
"v_mfma_f32_16x16x32_bf16 v[12:15], v[172:175], v[128:131], v[12:15]\n\t"
"v_mfma_f32_16x16x32_bf16 v[44:47], v[172:175], v[136:139], v[44:47]\n\t"
"ds_read_b128 v[128:131], v229 offset:0\n\t"
"v_mfma_f32_16x16x32_bf16 v[60:63], v[172:175], v[140:143], v[60:63]\n\t"
"ds_read_b128 v[132:135], v229 offset:2048\n\t"
"v_mfma_f32_16x16x32_bf16 v[56:59], v[168:171], v[140:143], v[56:59]\n\t"
"v_mfma_f32_16x16x32_bf16 v[40:43], v[168:171], v[136:139], v[40:43]\n\t"
"v_mfma_f32_16x16x32_bf16 v[36:39], v[164:167], v[136:139], v[36:39]\n\t"
"v_mfma_f32_16x16x32_bf16 v[52:55], v[164:167], v[140:143], v[52:55]\n\t"
"v_mfma_f32_16x16x32_bf16 v[48:51], v[160:163], v[140:143], v[48:51]\n\t"
"v_mfma_f32_16x16x32_bf16 v[32:35], v[160:163], v[136:139], v[32:35]\n\t"
"s_waitcnt lgkmcnt(2)\n\t"
"v_mfma_f32_16x16x32_bf16 v[64:67], v[160:163], v[144:147], v[64:67]\n\t"
"ds_read_b128 v[136:139], v229 offset:4096\n\t"
"v_mfma_f32_16x16x32_bf16 v[80:83], v[160:163], v[148:151], v[80:83]\n\t"
"ds_read_b128 v[140:143], v229 offset:6144\n\t"
"v_mfma_f32_16x16x32_bf16 v[84:87], v[164:167], v[148:151], v[84:87]\n\t"
"v_mfma_f32_16x16x32_bf16 v[68:71], v[164:167], v[144:147], v[68:71]\n\t"
"v_mfma_f32_16x16x32_bf16 v[72:75], v[168:171], v[144:147], v[72:75]\n\t"
"v_mfma_f32_16x16x32_bf16 v[88:91], v[168:171], v[148:151], v[88:91]\n\t"
"v_mfma_f32_16x16x32_bf16 v[92:95], v[172:175], v[148:151], v[92:95]\n\t"
"v_mfma_f32_16x16x32_bf16 v[76:79], v[172:175], v[144:147], v[76:79]\n\t"
"v_mfma_f32_16x16x32_bf16 v[108:111], v[172:175], v[152:155], v[108:111]\n\t"
"ds_read_b128 v[144:147], v229 offset:16384\n\t"
"v_mfma_f32_16x16x32_bf16 v[124:127], v[172:175], v[156:159], v[124:127]\n\t"
"ds_read_b128 v[148:151], v229 offset:18432\n\t"
"v_mfma_f32_16x16x32_bf16 v[120:123], v[168:171], v[156:159], v[120:123]\n\t"
"v_mfma_f32_16x16x32_bf16 v[104:107], v[168:171], v[152:155], v[104:107]\n\t"
"v_mfma_f32_16x16x32_bf16 v[100:103], v[164:167], v[152:155], v[100:103]\n\t"
"v_mfma_f32_16x16x32_bf16 v[116:119], v[164:167], v[156:159], v[116:119]\n\t"
"v_mfma_f32_16x16x32_bf16 v[112:115], v[160:163], v[156:159], v[112:115]\n\t"
"v_mfma_f32_16x16x32_bf16 v[96:99], v[160:163], v[152:155], v[96:99]\n\t"
"ds_read_b128 v[152:155], v229 offset:20480\n\t"
"s_waitcnt lgkmcnt(3)\n\t"
"v_mfma_f32_16x16x32_bf16 v[0:3], v[176:179], v[128:131], v[0:3]\n\t"
"ds_read_b128 v[156:159], v229 offset:22528\n\t"
"v_mfma_f32_16x16x32_bf16 v[16:19], v[176:179], v[132:135], v[16:19]\n\t"
"v_mfma_f32_16x16x32_bf16 v[20:23], v[180:183], v[132:135], v[20:23]\n\t"
"v_mfma_f32_16x16x32_bf16 v[4:7], v[180:183], v[128:131], v[4:7]\n\t"
"v_mfma_f32_16x16x32_bf16 v[8:11], v[184:187], v[128:131], v[8:11]\n\t"
"v_mfma_f32_16x16x32_bf16 v[24:27], v[184:187], v[132:135], v[24:27]\n\t"
"v_mfma_f32_16x16x32_bf16 v[28:31], v[188:191], v[132:135], v[28:31]\n\t"
"v_mfma_f32_16x16x32_bf16 v[12:15], v[188:191], v[128:131], v[12:15]\n\t"
"s_waitcnt lgkmcnt(0)\n\t"
"s_barrier\n\t"
"v_mfma_f32_16x16x32_bf16 v[44:47], v[188:191], v[136:139], v[44:47]\n\t"
"ds_read_b128 v[160:163], v250 offset:32768\n\t"
"v_mfma_f32_16x16x32_bf16 v[60:63], v[188:191], v[140:143], v[60:63]\n\t"
"ds_read_b128 v[164:167], v250 offset:34816\n\t"
"v_mfma_f32_16x16x32_bf16 v[56:59], v[184:187], v[140:143], v[56:59]\n\t"
"ds_read_b128 v[168:171], v250 offset:40960\n\t"
"v_mfma_f32_16x16x32_bf16 v[40:43], v[184:187], v[136:139], v[40:43]\n\t"
"ds_read_b128 v[172:175], v250 offset:43008\n\t"
"v_mfma_f32_16x16x32_bf16 v[36:39], v[180:183], v[136:139], v[36:39]\n\t"
"ds_read_b128 v[128:131], v248 offset:0\n\t"
"v_mfma_f32_16x16x32_bf16 v[52:55], v[180:183], v[140:143], v[52:55]\n\t"
"ds_read_b128 v[132:135], v248 offset:2048\n\t"
"v_mfma_f32_16x16x32_bf16 v[48:51], v[176:179], v[140:143], v[48:51]\n\t"
"v_mfma_f32_16x16x32_bf16 v[32:35], v[176:179], v[136:139], v[32:35]\n\t"
"v_mfma_f32_16x16x32_bf16 v[64:67], v[176:179], v[144:147], v[64:67]\n\t"
"ds_read_b128 v[136:139], v248 offset:4096\n\t"
"v_mfma_f32_16x16x32_bf16 v[80:83], v[176:179], v[148:151], v[80:83]\n\t"
"ds_read_b128 v[140:143], v248 offset:6144\n\t"
"v_mfma_f32_16x16x32_bf16 v[84:87], v[180:183], v[148:151], v[84:87]\n\t"
"v_mfma_f32_16x16x32_bf16 v[68:71], v[180:183], v[144:147], v[68:71]\n\t"
"v_mfma_f32_16x16x32_bf16 v[72:75], v[184:187], v[144:147], v[72:75]\n\t"
"v_mfma_f32_16x16x32_bf16 v[88:91], v[184:187], v[148:151], v[88:91]\n\t"
"v_mfma_f32_16x16x32_bf16 v[92:95], v[188:191], v[148:151], v[92:95]\n\t"
"v_mfma_f32_16x16x32_bf16 v[76:79], v[188:191], v[144:147], v[76:79]\n\t"
"v_mfma_f32_16x16x32_bf16 v[108:111], v[188:191], v[152:155], v[108:111]\n\t"
"ds_read_b128 v[144:147], v248 offset:16384\n\t"
"v_mfma_f32_16x16x32_bf16 v[124:127], v[188:191], v[156:159], v[124:127]\n\t"
"ds_read_b128 v[148:151], v248 offset:18432\n\t"
"v_mfma_f32_16x16x32_bf16 v[120:123], v[184:187], v[156:159], v[120:123]\n\t"
"v_mfma_f32_16x16x32_bf16 v[104:107], v[184:187], v[152:155], v[104:107]\n\t"
"v_mfma_f32_16x16x32_bf16 v[100:103], v[180:183], v[152:155], v[100:103]\n\t"
"v_mfma_f32_16x16x32_bf16 v[116:119], v[180:183], v[156:159], v[116:119]\n\t"
"v_mfma_f32_16x16x32_bf16 v[112:115], v[176:179], v[156:159], v[112:115]\n\t"
"v_mfma_f32_16x16x32_bf16 v[96:99], v[176:179], v[152:155], v[96:99]\n\t"
"ds_read_b128 v[152:155], v248 offset:20480\n\t"
"s_waitcnt lgkmcnt(3)\n\t"
"v_mfma_f32_16x16x32_bf16 v[0:3], v[160:163], v[128:131], v[0:3]\n\t"
"ds_read_b128 v[156:159], v248 offset:22528\n\t"
"v_mfma_f32_16x16x32_bf16 v[16:19], v[160:163], v[132:135], v[16:19]\n\t"
"ds_read_b128 v[176:179], v250 offset:33792\n\t"
"v_mfma_f32_16x16x32_bf16 v[20:23], v[164:167], v[132:135], v[20:23]\n\t"
"ds_read_b128 v[180:183], v250 offset:35840\n\t"
"v_mfma_f32_16x16x32_bf16 v[4:7], v[164:167], v[128:131], v[4:7]\n\t"
"ds_read_b128 v[184:187], v250 offset:41984\n\t"
"v_mfma_f32_16x16x32_bf16 v[8:11], v[168:171], v[128:131], v[8:11]\n\t"
"ds_read_b128 v[188:191], v250 offset:44032\n\t"
"v_mfma_f32_16x16x32_bf16 v[24:27], v[168:171], v[132:135], v[24:27]\n\t"
"v_mfma_f32_16x16x32_bf16 v[28:31], v[172:175], v[132:135], v[28:31]\n\t"
"v_mfma_f32_16x16x32_bf16 v[12:15], v[172:175], v[128:131], v[12:15]\n\t"
"v_mfma_f32_16x16x32_bf16 v[44:47], v[172:175], v[136:139], v[44:47]\n\t"
"ds_read_b128 v[128:131], v249 offset:0\n\t"
"v_mfma_f32_16x16x32_bf16 v[60:63], v[172:175], v[140:143], v[60:63]\n\t"
"ds_read_b128 v[132:135], v249 offset:2048\n\t"
"v_mfma_f32_16x16x32_bf16 v[56:59], v[168:171], v[140:143], v[56:59]\n\t"
"v_mfma_f32_16x16x32_bf16 v[40:43], v[168:171], v[136:139], v[40:43]\n\t"
"v_mfma_f32_16x16x32_bf16 v[36:39], v[164:167], v[136:139], v[36:39]\n\t"
"v_mfma_f32_16x16x32_bf16 v[52:55], v[164:167], v[140:143], v[52:55]\n\t"
"v_mfma_f32_16x16x32_bf16 v[48:51], v[160:163], v[140:143], v[48:51]\n\t"
"v_mfma_f32_16x16x32_bf16 v[32:35], v[160:163], v[136:139], v[32:35]\n\t"
"s_waitcnt lgkmcnt(2)\n\t"
"v_mfma_f32_16x16x32_bf16 v[64:67], v[160:163], v[144:147], v[64:67]\n\t"
"ds_read_b128 v[136:139], v249 offset:4096\n\t"
"v_mfma_f32_16x16x32_bf16 v[80:83], v[160:163], v[148:151], v[80:83]\n\t"
"ds_read_b128 v[140:143], v249 offset:6144\n\t"
"v_mfma_f32_16x16x32_bf16 v[84:87], v[164:167], v[148:151], v[84:87]\n\t"
"v_mfma_f32_16x16x32_bf16 v[68:71], v[164:167], v[144:147], v[68:71]\n\t"
"v_mfma_f32_16x16x32_bf16 v[72:75], v[168:171], v[144:147], v[72:75]\n\t"
"v_mfma_f32_16x16x32_bf16 v[88:91], v[168:171], v[148:151], v[88:91]\n\t"
"v_mfma_f32_16x16x32_bf16 v[92:95], v[172:175], v[148:151], v[92:95]\n\t"
"v_mfma_f32_16x16x32_bf16 v[76:79], v[172:175], v[144:147], v[76:79]\n\t"
"v_mfma_f32_16x16x32_bf16 v[108:111], v[172:175], v[152:155], v[108:111]\n\t"
"ds_read_b128 v[144:147], v249 offset:16384\n\t"
"v_mfma_f32_16x16x32_bf16 v[124:127], v[172:175], v[156:159], v[124:127]\n\t"
"ds_read_b128 v[148:151], v249 offset:18432\n\t"
"v_mfma_f32_16x16x32_bf16 v[120:123], v[168:171], v[156:159], v[120:123]\n\t"
"v_mfma_f32_16x16x32_bf16 v[104:107], v[168:171], v[152:155], v[104:107]\n\t"
"v_mfma_f32_16x16x32_bf16 v[100:103], v[164:167], v[152:155], v[100:103]\n\t"
"v_mfma_f32_16x16x32_bf16 v[116:119], v[164:167], v[156:159], v[116:119]\n\t"
"v_mfma_f32_16x16x32_bf16 v[112:115], v[160:163], v[156:159], v[112:115]\n\t"
"v_mfma_f32_16x16x32_bf16 v[96:99], v[160:163], v[152:155], v[96:99]\n\t"
"ds_read_b128 v[152:155], v249 offset:20480\n\t"
"s_waitcnt lgkmcnt(3)\n\t"
"v_mfma_f32_16x16x32_bf16 v[0:3], v[176:179], v[128:131], v[0:3]\n\t"
"ds_read_b128 v[156:159], v249 offset:22528\n\t"
"v_mfma_f32_16x16x32_bf16 v[16:19], v[176:179], v[132:135], v[16:19]\n\t"
"v_mfma_f32_16x16x32_bf16 v[20:23], v[180:183], v[132:135], v[20:23]\n\t"
"v_mfma_f32_16x16x32_bf16 v[4:7], v[180:183], v[128:131], v[4:7]\n\t"
"v_mfma_f32_16x16x32_bf16 v[8:11], v[184:187], v[128:131], v[8:11]\n\t"
"v_mfma_f32_16x16x32_bf16 v[24:27], v[184:187], v[132:135], v[24:27]\n\t"
"v_mfma_f32_16x16x32_bf16 v[28:31], v[188:191], v[132:135], v[28:31]\n\t"
"v_mfma_f32_16x16x32_bf16 v[12:15], v[188:191], v[128:131], v[12:15]\n\t"
"s_waitcnt lgkmcnt(0)\n\t"
"s_barrier\n\t"
"v_mfma_f32_16x16x32_bf16 v[44:47], v[188:191], v[136:139], v[44:47]\n\t"
"ds_read_b128 v[160:163], v250 offset:49152\n\t"
"v_mfma_f32_16x16x32_bf16 v[60:63], v[188:191], v[140:143], v[60:63]\n\t"
"ds_read_b128 v[164:167], v250 offset:51200\n\t"
"v_mfma_f32_16x16x32_bf16 v[56:59], v[184:187], v[140:143], v[56:59]\n\t"
"ds_read_b128 v[168:171], v250 offset:57344\n\t"
"v_mfma_f32_16x16x32_bf16 v[40:43], v[184:187], v[136:139], v[40:43]\n\t"
"ds_read_b128 v[172:175], v250 offset:59392\n\t"
"v_mfma_f32_16x16x32_bf16 v[36:39], v[180:183], v[136:139], v[36:39]\n\t"
"ds_read_b128 v[128:131], v248 offset:32768\n\t"
"v_mfma_f32_16x16x32_bf16 v[52:55], v[180:183], v[140:143], v[52:55]\n\t"
"ds_read_b128 v[132:135], v248 offset:34816\n\t"
"v_mfma_f32_16x16x32_bf16 v[48:51], v[176:179], v[140:143], v[48:51]\n\t"
"v_mfma_f32_16x16x32_bf16 v[32:35], v[176:179], v[136:139], v[32:35]\n\t"
"v_mfma_f32_16x16x32_bf16 v[64:67], v[176:179], v[144:147], v[64:67]\n\t"
"ds_read_b128 v[136:139], v248 offset:36864\n\t"
"v_mfma_f32_16x16x32_bf16 v[80:83], v[176:179], v[148:151], v[80:83]\n\t"
"ds_read_b128 v[140:143], v248 offset:38912\n\t"
"v_mfma_f32_16x16x32_bf16 v[84:87], v[180:183], v[148:151], v[84:87]\n\t"
"v_mfma_f32_16x16x32_bf16 v[68:71], v[180:183], v[144:147], v[68:71]\n\t"
"v_mfma_f32_16x16x32_bf16 v[72:75], v[184:187], v[144:147], v[72:75]\n\t"
"v_mfma_f32_16x16x32_bf16 v[88:91], v[184:187], v[148:151], v[88:91]\n\t"
"v_mfma_f32_16x16x32_bf16 v[92:95], v[188:191], v[148:151], v[92:95]\n\t"
"v_mfma_f32_16x16x32_bf16 v[76:79], v[188:191], v[144:147], v[76:79]\n\t"
"v_mfma_f32_16x16x32_bf16 v[108:111], v[188:191], v[152:155], v[108:111]\n\t"
"ds_read_b128 v[144:147], v248 offset:49152\n\t"
"v_mfma_f32_16x16x32_bf16 v[124:127], v[188:191], v[156:159], v[124:127]\n\t"
"ds_read_b128 v[148:151], v248 offset:51200\n\t"
"v_mfma_f32_16x16x32_bf16 v[120:123], v[184:187], v[156:159], v[120:123]\n\t"
"v_mfma_f32_16x16x32_bf16 v[104:107], v[184:187], v[152:155], v[104:107]\n\t"
"v_mfma_f32_16x16x32_bf16 v[100:103], v[180:183], v[152:155], v[100:103]\n\t"
"v_mfma_f32_16x16x32_bf16 v[116:119], v[180:183], v[156:159], v[116:119]\n\t"
"v_mfma_f32_16x16x32_bf16 v[112:115], v[176:179], v[156:159], v[112:115]\n\t"
"v_mfma_f32_16x16x32_bf16 v[96:99], v[176:179], v[152:155], v[96:99]\n\t"
"ds_read_b128 v[152:155], v248 offset:53248\n\t"
"s_waitcnt lgkmcnt(2)\n\t"
"v_mfma_f32_16x16x32_bf16 v[64:67], v[160:163], v[144:147], v[64:67]\n\t"
"ds_read_b128 v[156:159], v248 offset:55296\n\t"
"s_waitcnt lgkmcnt(2)\n\t"
"v_mfma_f32_16x16x32_bf16 v[80:83], v[160:163], v[148:151], v[80:83]\n\t"
"ds_read_b128 v[176:179], v250 offset:50176\n\t"
"v_mfma_f32_16x16x32_bf16 v[84:87], v[164:167], v[148:151], v[84:87]\n\t"
"ds_read_b128 v[180:183], v250 offset:52224\n\t"
"v_mfma_f32_16x16x32_bf16 v[68:71], v[164:167], v[144:147], v[68:71]\n\t"
"ds_read_b128 v[184:187], v250 offset:58368\n\t"
"v_mfma_f32_16x16x32_bf16 v[72:75], v[168:171], v[144:147], v[72:75]\n\t"
"ds_read_b128 v[188:191], v250 offset:60416\n\t"
"v_mfma_f32_16x16x32_bf16 v[88:91], v[168:171], v[148:151], v[88:91]\n\t"
"v_mfma_f32_16x16x32_bf16 v[92:95], v[172:175], v[148:151], v[92:95]\n\t"
"v_mfma_f32_16x16x32_bf16 v[76:79], v[172:175], v[144:147], v[76:79]\n\t"
"s_waitcnt lgkmcnt(5)\n\t"
"v_mfma_f32_16x16x32_bf16 v[108:111], v[172:175], v[152:155], v[108:111]\n\t"
"ds_read_b128 v[144:147], v249 offset:49152\n\t"
"s_waitcnt lgkmcnt(5)\n\t"
"v_mfma_f32_16x16x32_bf16 v[124:127], v[172:175], v[156:159], v[124:127]\n\t"
"ds_read_b128 v[148:151], v249 offset:51200\n\t"
"v_mfma_f32_16x16x32_bf16 v[120:123], v[168:171], v[156:159], v[120:123]\n\t"
"v_mfma_f32_16x16x32_bf16 v[104:107], v[168:171], v[152:155], v[104:107]\n\t"
"v_mfma_f32_16x16x32_bf16 v[100:103], v[164:167], v[152:155], v[100:103]\n\t"
"v_mfma_f32_16x16x32_bf16 v[116:119], v[164:167], v[156:159], v[116:119]\n\t"
"v_mfma_f32_16x16x32_bf16 v[112:115], v[160:163], v[156:159], v[112:115]\n\t"
"v_mfma_f32_16x16x32_bf16 v[96:99], v[160:163], v[152:155], v[96:99]\n\t"
"v_mfma_f32_16x16x32_bf16 v[0:3], v[160:163], v[128:131], v[0:3]\n\t"
"ds_read_b128 v[152:155], v249 offset:53248\n\t"
"v_mfma_f32_16x16x32_bf16 v[16:19], v[160:163], v[132:135], v[16:19]\n\t"
"ds_read_b128 v[156:159], v249 offset:55296\n\t"
"v_mfma_f32_16x16x32_bf16 v[20:23], v[164:167], v[132:135], v[20:23]\n\t"
"v_mfma_f32_16x16x32_bf16 v[4:7], v[164:167], v[128:131], v[4:7]\n\t"
"v_mfma_f32_16x16x32_bf16 v[8:11], v[168:171], v[128:131], v[8:11]\n\t"
"v_mfma_f32_16x16x32_bf16 v[24:27], v[168:171], v[132:135], v[24:27]\n\t"
"v_mfma_f32_16x16x32_bf16 v[28:31], v[172:175], v[132:135], v[28:31]\n\t"
"v_mfma_f32_16x16x32_bf16 v[12:15], v[172:175], v[128:131], v[12:15]\n\t"
"v_mfma_f32_16x16x32_bf16 v[44:47], v[172:175], v[136:139], v[44:47]\n\t"
"ds_read_b128 v[128:131], v249 offset:32768\n\t"
"v_mfma_f32_16x16x32_bf16 v[60:63], v[172:175], v[140:143], v[60:63]\n\t"
"ds_read_b128 v[132:135], v249 offset:34816\n\t"
"v_mfma_f32_16x16x32_bf16 v[56:59], v[168:171], v[140:143], v[56:59]\n\t"
"v_mfma_f32_16x16x32_bf16 v[40:43], v[168:171], v[136:139], v[40:43]\n\t"
"v_mfma_f32_16x16x32_bf16 v[36:39], v[164:167], v[136:139], v[36:39]\n\t"
"v_mfma_f32_16x16x32_bf16 v[52:55], v[164:167], v[140:143], v[52:55]\n\t"
"v_mfma_f32_16x16x32_bf16 v[48:51], v[160:163], v[140:143], v[48:51]\n\t"
"v_mfma_f32_16x16x32_bf16 v[32:35], v[160:163], v[136:139], v[32:35]\n\t"
"ds_read_b128 v[136:139], v249 offset:36864\n\t"
"s_waitcnt lgkmcnt(3)\n\t"
"v_mfma_f32_16x16x32_bf16 v[64:67], v[176:179], v[144:147], v[64:67]\n\t"
"ds_read_b128 v[140:143], v249 offset:38912\n\t"
"v_mfma_f32_16x16x32_bf16 v[80:83], v[176:179], v[148:151], v[80:83]\n\t"
"v_mfma_f32_16x16x32_bf16 v[84:87], v[180:183], v[148:151], v[84:87]\n\t"
"v_mfma_f32_16x16x32_bf16 v[68:71], v[180:183], v[144:147], v[68:71]\n\t"
"v_mfma_f32_16x16x32_bf16 v[72:75], v[184:187], v[144:147], v[72:75]\n\t"
"v_mfma_f32_16x16x32_bf16 v[88:91], v[184:187], v[148:151], v[88:91]\n\t"
"v_mfma_f32_16x16x32_bf16 v[92:95], v[188:191], v[148:151], v[92:95]\n\t"
"v_mfma_f32_16x16x32_bf16 v[76:79], v[188:191], v[144:147], v[76:79]\n\t"
"s_waitcnt lgkmcnt(0)\n\t"
"s_barrier\n\t"
"v_mfma_f32_16x16x32_bf16 v[108:111], v[188:191], v[152:155], v[108:111]\n\t"
"v_mfma_f32_16x16x32_bf16 v[124:127], v[188:191], v[156:159], v[124:127]\n\t"
"v_mfma_f32_16x16x32_bf16 v[120:123], v[184:187], v[156:159], v[120:123]\n\t"
"v_mfma_f32_16x16x32_bf16 v[104:107], v[184:187], v[152:155], v[104:107]\n\t"
"v_mfma_f32_16x16x32_bf16 v[100:103], v[180:183], v[152:155], v[100:103]\n\t"
"v_mfma_f32_16x16x32_bf16 v[116:119], v[180:183], v[156:159], v[116:119]\n\t"
"v_mfma_f32_16x16x32_bf16 v[112:115], v[176:179], v[156:159], v[112:115]\n\t"
"v_mfma_f32_16x16x32_bf16 v[96:99], v[176:179], v[152:155], v[96:99]\n\t"
"v_mfma_f32_16x16x32_bf16 v[0:3], v[176:179], v[128:131], v[0:3]\n\t"
"v_cvt_pk_bf16_f32 v64, v64, v65\n\t"
"v_cvt_pk_bf16_f32 v65, v66, v67\n\t"
"v_mfma_f32_16x16x32_bf16 v[16:19], v[176:179], v[132:135], v[16:19]\n\t"
"v_cvt_pk_bf16_f32 v66, v68, v69\n\t"
"v_cvt_pk_bf16_f32 v67, v70, v71\n\t"
"v_mfma_f32_16x16x32_bf16 v[20:23], v[180:183], v[132:135], v[20:23]\n\t"
"v_min_u32 v64, -2, v64\n\t"
"v_min_u32 v65, -2, v65\n\t"
"v_mfma_f32_16x16x32_bf16 v[4:7], v[180:183], v[128:131], v[4:7]\n\t"
"v_min_u32 v66, -2, v66\n\t"
"v_min_u32 v67, -2, v67\n\t"
"v_mfma_f32_16x16x32_bf16 v[8:11], v[184:187], v[128:131], v[8:11]\n\t"
"buffer_store_dwordx4 v[64:67], v227, s[24:27], s84 offen sc1\n\t"
"v_cvt_pk_bf16_f32 v80, v80, v81\n\t"
"v_mfma_f32_16x16x32_bf16 v[24:27], v[184:187], v[132:135], v[24:27]\n\t"
"v_cvt_pk_bf16_f32 v81, v82, v83\n\t"
"v_cvt_pk_bf16_f32 v82, v84, v85\n\t"
"v_mfma_f32_16x16x32_bf16 v[28:31], v[188:191], v[132:135], v[28:31]\n\t"
"v_cvt_pk_bf16_f32 v83, v86, v87\n\t"
"v_min_u32 v80, -2, v80\n\t"
"v_mfma_f32_16x16x32_bf16 v[12:15], v[188:191], v[128:131], v[12:15]\n\t"
"v_min_u32 v81, -2, v81\n\t"
"v_min_u32 v82, -2, v82\n\t"
"v_mfma_f32_16x16x32_bf16 v[44:47], v[188:191], v[136:139], v[44:47]\n\t"
"v_min_u32 v83, -2, v83\n\t"
"buffer_store_dwordx4 v[80:83], v227, s[24:27], s84 offen offset:1024 sc1\n\t"
"v_mfma_f32_16x16x32_bf16 v[60:63], v[188:191], v[140:143], v[60:63]\n\t"
"v_cvt_pk_bf16_f32 v96, v96, v97\n\t"
"v_cvt_pk_bf16_f32 v97, v98, v99\n\t"
"v_mfma_f32_16x16x32_bf16 v[56:59], v[184:187], v[140:143], v[56:59]\n\t"
"v_cvt_pk_bf16_f32 v98, v100, v101\n\t"
"v_cvt_pk_bf16_f32 v99, v102, v103\n\t"
"v_mfma_f32_16x16x32_bf16 v[40:43], v[184:187], v[136:139], v[40:43]\n\t"
"v_min_u32 v96, -2, v96\n\t"
"v_min_u32 v97, -2, v97\n\t"
"v_mfma_f32_16x16x32_bf16 v[36:39], v[180:183], v[136:139], v[36:39]\n\t"
"v_min_u32 v98, -2, v98\n\t"
"v_min_u32 v99, -2, v99\n\t"
"v_mfma_f32_16x16x32_bf16 v[52:55], v[180:183], v[140:143], v[52:55]\n\t"
"buffer_store_dwordx4 v[96:99], v227, s[24:27], s84 offen offset:2048 sc1\n\t"
"v_mfma_f32_16x16x32_bf16 v[48:51], v[176:179], v[140:143], v[48:51]\n\t"
"v_mfma_f32_16x16x32_bf16 v[32:35], v[176:179], v[136:139], v[32:35]\n\t"
"s_setprio 0\n\t"
"s_lshl_b32 s88, s45, 12\n\t"
"s_add_u32 s88, s88, s87\n\t"
"s_add_u32 s88, s88, 1020\n\t"
"v_cvt_pk_bf16_f32 v112, v112, v113\n\t"
"v_cvt_pk_bf16_f32 v113, v114, v115\n\t"
"v_cvt_pk_bf16_f32 v114, v116, v117\n\t"
"v_cvt_pk_bf16_f32 v115, v118, v119\n\t"
"v_min_u32 v112, -2, v112\n\t"
"v_min_u32 v113, -2, v113\n\t"
"v_min_u32 v114, -2, v114\n\t"
"v_min_u32 v115, -2, v115\n\t"
"buffer_store_dwordx4 v[112:115], v227, s[24:27], s84 offen offset:3072 sc1\n\t"
"v_cvt_pk_bf16_f32 v72, v72, v73\n\t"
"v_cvt_pk_bf16_f32 v73, v74, v75\n\t"
"v_cvt_pk_bf16_f32 v74, v76, v77\n\t"
"v_cvt_pk_bf16_f32 v75, v78, v79\n\t"
"v_min_u32 v72, -2, v72\n\t"
"v_min_u32 v73, -2, v73\n\t"
"v_min_u32 v74, -2, v74\n\t"
"v_min_u32 v75, -2, v75\n\t"
"buffer_store_dwordx4 v[72:75], v227, s[24:27], s86 offen sc1\n\t"
"v_cvt_pk_bf16_f32 v88, v88, v89\n\t"
"v_cvt_pk_bf16_f32 v89, v90, v91\n\t"
"v_cvt_pk_bf16_f32 v90, v92, v93\n\t"
"v_cvt_pk_bf16_f32 v91, v94, v95\n\t"
"v_min_u32 v88, -2, v88\n\t"
"v_min_u32 v89, -2, v89\n\t"
"v_min_u32 v90, -2, v90\n\t"
"v_min_u32 v91, -2, v91\n\t"
"buffer_store_dwordx4 v[88:91], v227, s[24:27], s86 offen offset:1024 sc1\n\t"
"v_cvt_pk_bf16_f32 v104, v104, v105\n\t"
"v_cvt_pk_bf16_f32 v105, v106, v107\n\t"
"v_cvt_pk_bf16_f32 v106, v108, v109\n\t"
"v_cvt_pk_bf16_f32 v107, v110, v111\n\t"
"v_min_u32 v104, -2, v104\n\t"
"v_min_u32 v105, -2, v105\n\t"
"v_min_u32 v106, -2, v106\n\t"
"v_min_u32 v107, -2, v107\n\t"
"buffer_store_dwordx4 v[104:107], v227, s[24:27], s86 offen offset:2048 sc1\n\t"
"v_cvt_pk_bf16_f32 v120, v120, v121\n\t"
"v_cvt_pk_bf16_f32 v121, v122, v123\n\t"
"v_cvt_pk_bf16_f32 v122, v124, v125\n\t"
"v_cvt_pk_bf16_f32 v123, v126, v127\n\t"
"v_min_u32 v120, -2, v120\n\t"
"v_min_u32 v121, -2, v121\n\t"
"v_min_u32 v122, -2, v122\n\t"
"v_min_u32 v123, -2, v123\n\t"
"buffer_store_dwordx4 v[120:123], v227, s[24:27], s86 offen offset:3072 sc1\n\t"
"v_cvt_pk_bf16_f32 v8, v8, v9\n\t"
"v_cvt_pk_bf16_f32 v9, v10, v11\n\t"
"v_cvt_pk_bf16_f32 v10, v12, v13\n\t"
"v_cvt_pk_bf16_f32 v11, v14, v15\n\t"
"v_min_u32 v8, -2, v8\n\t"
"v_min_u32 v9, -2, v9\n\t"
"v_min_u32 v10, -2, v10\n\t"
"v_min_u32 v11, -2, v11\n\t"
"buffer_store_dwordx4 v[8:11], v227, s[24:27], s85 offen sc1\n\t"
"v_cvt_pk_bf16_f32 v24, v24, v25\n\t"
"v_cvt_pk_bf16_f32 v25, v26, v27\n\t"
"v_cvt_pk_bf16_f32 v26, v28, v29\n\t"
"v_cvt_pk_bf16_f32 v27, v30, v31\n\t"
"v_min_u32 v24, -2, v24\n\t"
"v_min_u32 v25, -2, v25\n\t"
"v_min_u32 v26, -2, v26\n\t"
"v_min_u32 v27, -2, v27\n\t"
"buffer_store_dwordx4 v[24:27], v227, s[24:27], s85 offen offset:1024 sc1\n\t"
"v_cvt_pk_bf16_f32 v40, v40, v41\n\t"
"v_cvt_pk_bf16_f32 v41, v42, v43\n\t"
"v_cvt_pk_bf16_f32 v42, v44, v45\n\t"
"v_cvt_pk_bf16_f32 v43, v46, v47\n\t"
"v_min_u32 v40, -2, v40\n\t"
"v_min_u32 v41, -2, v41\n\t"
"v_min_u32 v42, -2, v42\n\t"
"v_min_u32 v43, -2, v43\n\t"
"s_buffer_load_dword s95, s[24:27], s88 offset:0x0 glc\n\t"
"s_buffer_load_dword s96, s[24:27], s88 offset:0x4000 glc\n\t"
"s_buffer_load_dword s97, s[24:27], s88 offset:0x8000 glc\n\t"
"buffer_store_dwordx4 v[40:43], v227, s[24:27], s85 offen offset:2048 sc1\n\t"
"v_cvt_pk_bf16_f32 v56, v56, v57\n\t"
"v_cvt_pk_bf16_f32 v57, v58, v59\n\t"
"v_cvt_pk_bf16_f32 v58, v60, v61\n\t"
"v_cvt_pk_bf16_f32 v59, v62, v63\n\t"
"v_min_u32 v56, -2, v56\n\t"
"v_min_u32 v57, -2, v57\n\t"
"v_min_u32 v58, -2, v58\n\t"
"v_min_u32 v59, -2, v59\n\t"
"buffer_store_dwordx4 v[56:59], v227, s[24:27], s85 offen offset:3072 sc1\n\t"
"s_add_u32 s91, s87, 16384\n\t"
"s_add_u32 s92, s87, 32768\n\t"
"s_mov_b32 s93, 0x3f80\n\t"
"s_mov_b32 s94, 0x3f800000\n\t"
"s_mov_b32 s78, 0x4000\n\t"
"L_xl_%=:\n\t"
"s_waitcnt lgkmcnt(0)\n\t"
"s_max_u32 s95, s95, s96\n\t"
"s_max_u32 s95, s95, s97\n\t"
"s_cmp_eq_u32 s95, -1\n\t"
"s_cbranch_scc0 L_xget_%=\n\t"
"s_buffer_load_dword s95, s[24:27], s88 offset:0x0 glc\n\t"
"s_buffer_load_dword s96, s[24:27], s88 offset:0x4000 glc\n\t"
"s_buffer_load_dword s97, s[24:27], s88 offset:0x8000 glc\n\t"
"s_sub_u32 s78, s78, 1\n\t"
"s_cmp_lg_u32 s78, 0\n\t"
"s_cbranch_scc1 L_xl_%=\n\t"
"s_waitcnt lgkmcnt(0)\n\t"
"L_xget_%=:\n\t"
"s_mov_b32 s78, 0x400000\n\t"
"buffer_load_dwordx4 v[80:83], v227, s[24:27], s91 offen offset:0 sc1\n\t"
"buffer_load_dwordx4 v[64:67], v227, s[24:27], s87 offen offset:0 sc1\n\t"
"buffer_load_dwordx4 v[96:99], v227, s[24:27], s92 offen offset:0 sc1\n\t"
"buffer_load_dwordx4 v[84:87], v227, s[24:27], s91 offen offset:1024 sc1\n\t"
"buffer_load_dwordx4 v[68:71], v227, s[24:27], s87 offen offset:1024 sc1\n\t"
"buffer_load_dwordx4 v[100:103], v227, s[24:27], s92 offen offset:1024 sc1\n\t"
"buffer_load_dwordx4 v[88:91], v227, s[24:27], s91 offen offset:2048 sc1\n\t"
"buffer_load_dwordx4 v[72:75], v227, s[24:27], s87 offen offset:2048 sc1\n\t"
"buffer_load_dwordx4 v[104:107], v227, s[24:27], s92 offen offset:2048 sc1\n\t"
"buffer_load_dwordx4 v[92:95], v227, s[24:27], s91 offen offset:3072 sc1\n\t"
"buffer_load_dwordx4 v[76:79], v227, s[24:27], s87 offen offset:3072 sc1\n\t"
"buffer_load_dwordx4 v[108:111], v227, s[24:27], s92 offen offset:3072 sc1\n\t"
"s_barrier\n\t"
"ds_read_b128 v[128:131], v226 offset:0\n\t"
"ds_read_b128 v[132:135], v226 offset:1024\n\t"
"ds_read_b128 v[136:139], v226 offset:4096\n\t"
"ds_read_b128 v[140:143], v226 offset:5120\n\t"
"ds_read_b128 v[144:147], v226 offset:8192\n\t"
"ds_read_b128 v[148:151], v226 offset:9216\n\t"
"ds_read_b128 v[152:155], v226 offset:12288\n\t"
"ds_read_b128 v[156:159], v226 offset:13312\n\t"
"s_waitcnt lgkmcnt(6)\n\t"
"v_add_f32 v0, v0, v128\n\t"
"v_add_f32 v1, v1, v129\n\t"
"v_add_f32 v2, v2, v130\n\t"
"v_add_f32 v3, v3, v131\n\t"
"v_add_f32 v4, v4, v132\n\t"
"v_add_f32 v5, v5, v133\n\t"
"v_add_f32 v6, v6, v134\n\t"
"v_add_f32 v7, v7, v135\n\t"
"s_waitcnt lgkmcnt(4)\n\t"
"v_add_f32 v16, v16, v136\n\t"
"v_add_f32 v17, v17, v137\n\t"
"v_add_f32 v18, v18, v138\n\t"
"v_add_f32 v19, v19, v139\n\t"
"v_add_f32 v20, v20, v140\n\t"
"v_add_f32 v21, v21, v141\n\t"
"v_add_f32 v22, v22, v142\n\t"
"v_add_f32 v23, v23, v143\n\t"
"s_waitcnt lgkmcnt(2)\n\t"
"v_add_f32 v32, v32, v144\n\t"
"v_add_f32 v33, v33, v145\n\t"
"v_add_f32 v34, v34, v146\n\t"
"v_add_f32 v35, v35, v147\n\t"
"v_add_f32 v36, v36, v148\n\t"
"v_add_f32 v37, v37, v149\n\t"
"v_add_f32 v38, v38, v150\n\t"
"v_add_f32 v39, v39, v151\n\t"
"s_waitcnt lgkmcnt(0)\n\t"
"v_add_f32 v48, v48, v152\n\t"
"v_add_f32 v49, v49, v153\n\t"
"v_add_f32 v50, v50, v154\n\t"
"v_add_f32 v51, v51, v155\n\t"
"v_add_f32 v52, v52, v156\n\t"
"v_add_f32 v53, v53, v157\n\t"
"v_add_f32 v54, v54, v158\n\t"
"v_add_f32 v55, v55, v159\n\t"
"L_sum_%=:\n\t"
"s_waitcnt vmcnt(9)\n\t"
"L_xc0_%=:\n\t"
"v_max3_u32 v240, v64, v65, v66\n\t"
"v_max3_u32 v241, v67, v80, v81\n\t"
"v_max3_u32 v240, v240, v82, v83\n\t"
"v_max3_u32 v241, v241, v96, v97\n\t"
"v_max3_u32 v240, v240, v98, v99\n\t"
"v_max_u32 v240, v240, v241\n\t"
"v_cmp_eq_u32 vcc, -1, v240\n\t"
"s_nop 4\n\t"
"s_cbranch_vccnz L_xr0_%=\n\t"
"L_xs0_%=:\n\t"
"v_dot2c_f32_bf16 v0, s93, v80\n\t"
"v_dot2c_f32_bf16 v1, s94, v80\n\t"
"v_dot2c_f32_bf16 v2, s93, v81\n\t"
"v_dot2c_f32_bf16 v3, s94, v81\n\t"
"v_dot2c_f32_bf16 v4, s93, v82\n\t"
"v_dot2c_f32_bf16 v5, s94, v82\n\t"
"v_dot2c_f32_bf16 v6, s93, v83\n\t"
"v_dot2c_f32_bf16 v7, s94, v83\n\t"
"v_dot2c_f32_bf16 v0, s93, v64\n\t"
"v_dot2c_f32_bf16 v1, s94, v64\n\t"
"v_dot2c_f32_bf16 v2, s93, v65\n\t"
"v_dot2c_f32_bf16 v3, s94, v65\n\t"
"v_dot2c_f32_bf16 v4, s93, v66\n\t"
"v_dot2c_f32_bf16 v5, s94, v66\n\t"
"v_dot2c_f32_bf16 v6, s93, v67\n\t"
"v_dot2c_f32_bf16 v7, s94, v67\n\t"
"v_dot2c_f32_bf16 v0, s93, v96\n\t"
"v_dot2c_f32_bf16 v1, s94, v96\n\t"
"v_dot2c_f32_bf16 v2, s93, v97\n\t"
"v_dot2c_f32_bf16 v3, s94, v97\n\t"
"v_dot2c_f32_bf16 v4, s93, v98\n\t"
"v_dot2c_f32_bf16 v5, s94, v98\n\t"
"v_dot2c_f32_bf16 v6, s93, v99\n\t"
"v_dot2c_f32_bf16 v7, s94, v99\n\t"
"v_mul_f32 v0, v244, v0\n\t"
"v_mul_f32 v1, v244, v1\n\t"
"v_mul_f32 v2, v244, v2\n\t"
"v_mul_f32 v3, v244, v3\n\t"
"v_mul_f32 v4, v244, v4\n\t"
"v_mul_f32 v5, v244, v5\n\t"
"v_mul_f32 v6, v244, v6\n\t"
"v_mul_f32 v7, v244, v7\n\t"
"v_cvt_pk_bf16_f32 v112, v0, v1\n\t"
"v_cvt_pk_bf16_f32 v113, v2, v3\n\t"
"v_cvt_pk_bf16_f32 v114, v4, v5\n\t"
"v_cvt_pk_bf16_f32 v115, v6, v7\n\t"
"s_nop 1\n\t"
"v_permlane16_swap_b32 v112, v114\n\t"
"v_permlane16_swap_b32 v113, v115\n\t"
"buffer_store_dwordx4 v[112:115], v224, s[36:39], s90 offen\n\t"
"s_add_u32 s90, s90, 32768\n\t"
"s_waitcnt vmcnt(7)\n\t"
"L_xc1_%=:\n\t"
"v_max3_u32 v240, v68, v69, v70\n\t"
"v_max3_u32 v241, v71, v84, v85\n\t"
"v_max3_u32 v240, v240, v86, v87\n\t"
"v_max3_u32 v241, v241, v100, v101\n\t"
"v_max3_u32 v240, v240, v102, v103\n\t"
"v_max_u32 v240, v240, v241\n\t"
"v_cmp_eq_u32 vcc, -1, v240\n\t"
"s_nop 4\n\t"
"s_cbranch_vccnz L_xr1_%=\n\t"
"L_xs1_%=:\n\t"
"v_dot2c_f32_bf16 v16, s93, v84\n\t"
"v_dot2c_f32_bf16 v17, s94, v84\n\t"
"v_dot2c_f32_bf16 v18, s93, v85\n\t"
"v_dot2c_f32_bf16 v19, s94, v85\n\t"
"v_dot2c_f32_bf16 v20, s93, v86\n\t"
"v_dot2c_f32_bf16 v21, s94, v86\n\t"
"v_dot2c_f32_bf16 v22, s93, v87\n\t"
"v_dot2c_f32_bf16 v23, s94, v87\n\t"
"v_dot2c_f32_bf16 v16, s93, v68\n\t"
"v_dot2c_f32_bf16 v17, s94, v68\n\t"
"v_dot2c_f32_bf16 v18, s93, v69\n\t"
"v_dot2c_f32_bf16 v19, s94, v69\n\t"
"v_dot2c_f32_bf16 v20, s93, v70\n\t"
"v_dot2c_f32_bf16 v21, s94, v70\n\t"
"v_dot2c_f32_bf16 v22, s93, v71\n\t"
"v_dot2c_f32_bf16 v23, s94, v71\n\t"
"v_dot2c_f32_bf16 v16, s93, v100\n\t"
"v_dot2c_f32_bf16 v17, s94, v100\n\t"
"v_dot2c_f32_bf16 v18, s93, v101\n\t"
"v_dot2c_f32_bf16 v19, s94, v101\n\t"
"v_dot2c_f32_bf16 v20, s93, v102\n\t"
"v_dot2c_f32_bf16 v21, s94, v102\n\t"
"v_dot2c_f32_bf16 v22, s93, v103\n\t"
"v_dot2c_f32_bf16 v23, s94, v103\n\t"
"v_mul_f32 v16, v245, v16\n\t"
"v_mul_f32 v17, v245, v17\n\t"
"v_mul_f32 v18, v245, v18\n\t"
"v_mul_f32 v19, v245, v19\n\t"
"v_mul_f32 v20, v245, v20\n\t"
"v_mul_f32 v21, v245, v21\n\t"
"v_mul_f32 v22, v245, v22\n\t"
"v_mul_f32 v23, v245, v23\n\t"
"v_cvt_pk_bf16_f32 v116, v16, v17\n\t"
"v_cvt_pk_bf16_f32 v117, v18, v19\n\t"
"v_cvt_pk_bf16_f32 v118, v20, v21\n\t"
"v_cvt_pk_bf16_f32 v119, v22, v23\n\t"
"s_nop 1\n\t"
"v_permlane16_swap_b32 v116, v118\n\t"
"v_permlane16_swap_b32 v117, v119\n\t"
"buffer_store_dwordx4 v[116:119], v224, s[36:39], s90 offen\n\t"
"s_add_u32 s90, s90, 32768\n\t"
"s_waitcnt vmcnt(5)\n\t"
"L_xc2_%=:\n\t"
"v_max3_u32 v240, v72, v73, v74\n\t"
"v_max3_u32 v241, v75, v88, v89\n\t"
"v_max3_u32 v240, v240, v90, v91\n\t"
"v_max3_u32 v241, v241, v104, v105\n\t"
"v_max3_u32 v240, v240, v106, v107\n\t"
"v_max_u32 v240, v240, v241\n\t"
"v_cmp_eq_u32 vcc, -1, v240\n\t"
"s_nop 4\n\t"
"s_cbranch_vccnz L_xr2_%=\n\t"
"L_xs2_%=:\n\t"
"v_dot2c_f32_bf16 v32, s93, v88\n\t"
"v_dot2c_f32_bf16 v33, s94, v88\n\t"
"v_dot2c_f32_bf16 v34, s93, v89\n\t"
"v_dot2c_f32_bf16 v35, s94, v89\n\t"
"v_dot2c_f32_bf16 v36, s93, v90\n\t"
"v_dot2c_f32_bf16 v37, s94, v90\n\t"
"v_dot2c_f32_bf16 v38, s93, v91\n\t"
"v_dot2c_f32_bf16 v39, s94, v91\n\t"
"v_dot2c_f32_bf16 v32, s93, v72\n\t"
"v_dot2c_f32_bf16 v33, s94, v72\n\t"
"v_dot2c_f32_bf16 v34, s93, v73\n\t"
"v_dot2c_f32_bf16 v35, s94, v73\n\t"
"v_dot2c_f32_bf16 v36, s93, v74\n\t"
"v_dot2c_f32_bf16 v37, s94, v74\n\t"
"v_dot2c_f32_bf16 v38, s93, v75\n\t"
"v_dot2c_f32_bf16 v39, s94, v75\n\t"
"v_dot2c_f32_bf16 v32, s93, v104\n\t"
"v_dot2c_f32_bf16 v33, s94, v104\n\t"
"v_dot2c_f32_bf16 v34, s93, v105\n\t"
"v_dot2c_f32_bf16 v35, s94, v105\n\t"
"v_dot2c_f32_bf16 v36, s93, v106\n\t"
"v_dot2c_f32_bf16 v37, s94, v106\n\t"
"v_dot2c_f32_bf16 v38, s93, v107\n\t"
"v_dot2c_f32_bf16 v39, s94, v107\n\t"
"v_mul_f32 v32, v246, v32\n\t"
"v_mul_f32 v33, v246, v33\n\t"
"v_mul_f32 v34, v246, v34\n\t"
"v_mul_f32 v35, v246, v35\n\t"
"v_mul_f32 v36, v246, v36\n\t"
"v_mul_f32 v37, v246, v37\n\t"
"v_mul_f32 v38, v246, v38\n\t"
"v_mul_f32 v39, v246, v39\n\t"
"v_cvt_pk_bf16_f32 v120, v32, v33\n\t"
"v_cvt_pk_bf16_f32 v121, v34, v35\n\t"
"v_cvt_pk_bf16_f32 v122, v36, v37\n\t"
"v_cvt_pk_bf16_f32 v123, v38, v39\n\t"
"s_nop 1\n\t"
"v_permlane16_swap_b32 v120, v122\n\t"
"v_permlane16_swap_b32 v121, v123\n\t"
"buffer_store_dwordx4 v[120:123], v224, s[36:39], s90 offen\n\t"
"s_add_u32 s90, s90, 32768\n\t"
"s_waitcnt vmcnt(3)\n\t"
"L_xc3_%=:\n\t"
"v_max3_u32 v240, v76, v77, v78\n\t"
"v_max3_u32 v241, v79, v92, v93\n\t"
"v_max3_u32 v240, v240, v94, v95\n\t"
"v_max3_u32 v241, v241, v108, v109\n\t"
"v_max3_u32 v240, v240, v110, v111\n\t"
"v_max_u32 v240, v240, v241\n\t"
"v_cmp_eq_u32 vcc, -1, v240\n\t"
"s_nop 4\n\t"
"s_cbranch_vccnz L_xr3_%=\n\t"
"L_xs3_%=:\n\t"
"v_dot2c_f32_bf16 v48, s93, v92\n\t"
"v_dot2c_f32_bf16 v49, s94, v92\n\t"
"v_dot2c_f32_bf16 v50, s93, v93\n\t"
"v_dot2c_f32_bf16 v51, s94, v93\n\t"
"v_dot2c_f32_bf16 v52, s93, v94\n\t"
"v_dot2c_f32_bf16 v53, s94, v94\n\t"
"v_dot2c_f32_bf16 v54, s93, v95\n\t"
"v_dot2c_f32_bf16 v55, s94, v95\n\t"
"v_dot2c_f32_bf16 v48, s93, v76\n\t"
"v_dot2c_f32_bf16 v49, s94, v76\n\t"
"v_dot2c_f32_bf16 v50, s93, v77\n\t"
"v_dot2c_f32_bf16 v51, s94, v77\n\t"
"v_dot2c_f32_bf16 v52, s93, v78\n\t"
"v_dot2c_f32_bf16 v53, s94, v78\n\t"
"v_dot2c_f32_bf16 v54, s93, v79\n\t"
"v_dot2c_f32_bf16 v55, s94, v79\n\t"
"v_dot2c_f32_bf16 v48, s93, v108\n\t"
"v_dot2c_f32_bf16 v49, s94, v108\n\t"
"v_dot2c_f32_bf16 v50, s93, v109\n\t"
"v_dot2c_f32_bf16 v51, s94, v109\n\t"
"v_dot2c_f32_bf16 v52, s93, v110\n\t"
"v_dot2c_f32_bf16 v53, s94, v110\n\t"
"v_dot2c_f32_bf16 v54, s93, v111\n\t"
"v_dot2c_f32_bf16 v55, s94, v111\n\t"
"v_mul_f32 v48, v247, v48\n\t"
"v_mul_f32 v49, v247, v49\n\t"
"v_mul_f32 v50, v247, v50\n\t"
"v_mul_f32 v51, v247, v51\n\t"
"v_mul_f32 v52, v247, v52\n\t"
"v_mul_f32 v53, v247, v53\n\t"
"v_mul_f32 v54, v247, v54\n\t"
"v_mul_f32 v55, v247, v55\n\t"
"v_cvt_pk_bf16_f32 v124, v48, v49\n\t"
"v_cvt_pk_bf16_f32 v125, v50, v51\n\t"
"v_cvt_pk_bf16_f32 v126, v52, v53\n\t"
"v_cvt_pk_bf16_f32 v127, v54, v55\n\t"
"s_nop 1\n\t"
"v_permlane16_swap_b32 v124, v126\n\t"
"v_permlane16_swap_b32 v125, v127\n\t"
"buffer_store_dwordx4 v[124:127], v224, s[36:39], s90 offen\n\t"
"s_add_u32 s90, s90, 32768\n\t"
"s_waitcnt vmcnt(0)\n\t"
"s_endpgm\n\t"
"L_fallback_%=:\n\t"
"s_waitcnt lgkmcnt(0)\n\t"
"v_and_b32 v241, 2, v253\n\t"
"v_cmp_eq_u32 vcc, 0, v241\n\t"
"v_mov_b32 v158, 0x44444444\n\t"
"v_mov_b32 v241, 0xeeeeeeee\n\t"
"v_and_b32 v240, 1, v253\n\t"
"v_cndmask_b32 v158, v241, v158, vcc\n\t"
"v_cmp_eq_u32 vcc, 0, v240\n\t"
"v_mov_b32 v241, 0x3f803f80\n\t"
"s_nop 0\n\t"
"v_cndmask_b32 v152, 0, v241, vcc\n\t"
"v_mov_b32 v153, v152\n\t"
"v_xor_b32 v154, v241, v152\n\t"
"v_xor_b32 v155, v241, v152\n\t"
"s_lshl_b32 s60, s47, 18\n\t"
"s_mov_b32 s56, 64\n\t"
"v_mov_b32 v240, 0xff800000\n\t"
"v_mov_b32 v241, 0xff800000\n\t"
"L_fb1_%=:\n\t"
"buffer_load_dwordx4 v[128:131], v238, s[24:27], s60 offen sc1\n\t"
"buffer_load_dwordx4 v[132:135], v238, s[24:27], s60 offen offset:1024 sc1\n\t"
"s_waitcnt vmcnt(0)\n\t"
"v_mfma_f32_32x32x16_bf16 v[32:47], v[128:131], v[96:99], 0\n\t"
"v_mfma_f32_32x32x16_bf16 v[48:63], v[128:131], v[104:107], 0\n\t"
"v_mfma_f32_32x32x16_bf16 v[32:47], v[132:135], v[100:103], v[32:47]\n\t"
"v_mfma_f32_32x32x16_bf16 v[48:63], v[132:135], v[108:111], v[48:63]\n\t"
"s_nop 15\n\t"
"v_max3_f32 v240, v240, v32, v33\n\t"
"v_max3_f32 v240, v240, v34, v35\n\t"
"v_max3_f32 v240, v240, v36, v37\n\t"
"v_max3_f32 v240, v240, v38, v39\n\t"
"v_max3_f32 v240, v240, v40, v41\n\t"
"v_max3_f32 v240, v240, v42, v43\n\t"
"v_max3_f32 v240, v240, v44, v45\n\t"
"v_max3_f32 v240, v240, v46, v47\n\t"
"v_max3_f32 v241, v241, v48, v49\n\t"
"v_max3_f32 v241, v241, v50, v51\n\t"
"v_max3_f32 v241, v241, v52, v53\n\t"
"v_max3_f32 v241, v241, v54, v55\n\t"
"v_max3_f32 v241, v241, v56, v57\n\t"
"v_max3_f32 v241, v241, v58, v59\n\t"
"v_max3_f32 v241, v241, v60, v61\n\t"
"v_max3_f32 v241, v241, v62, v63\n\t"
"s_add_u32 s60, s60, 4096\n\t"
"s_sub_u32 s56, s56, 1\n\t"
"s_cmp_lg_u32 s56, 0\n\t"
"s_cbranch_scc1 L_fb1_%=\n\t"
"v_mov_b32 v242, v240\n\t"
"s_nop 1\n\t"
"v_permlane32_swap_b32 v240, v242\n\t"
"s_nop 1\n\t"
"v_max_f32 v240, v240, v242\n\t"
"v_sub_f32 v64, 0, v240\n\t"
"v_mov_b32 v65, v64\n\t"
"v_mov_b32 v66, v64\n\t"
"v_mov_b32 v67, v64\n\t"
"v_mov_b32 v68, v64\n\t"
"v_mov_b32 v69, v64\n\t"
"v_mov_b32 v70, v64\n\t"
"v_mov_b32 v71, v64\n\t"
"v_mov_b32 v72, v64\n\t"
"v_mov_b32 v73, v64\n\t"
"v_mov_b32 v74, v64\n\t"
"v_mov_b32 v75, v64\n\t"
"v_mov_b32 v76, v64\n\t"
"v_mov_b32 v77, v64\n\t"
"v_mov_b32 v78, v64\n\t"
"v_mov_b32 v79, v64\n\t"
"v_mov_b32 v242, v241\n\t"
"s_nop 1\n\t"
"v_permlane32_swap_b32 v241, v242\n\t"
"s_nop 1\n\t"
"v_max_f32 v241, v241, v242\n\t"
"v_sub_f32 v80, 0, v241\n\t"
"v_mov_b32 v81, v80\n\t"
"v_mov_b32 v82, v80\n\t"
"v_mov_b32 v83, v80\n\t"
"v_mov_b32 v84, v80\n\t"
"v_mov_b32 v85, v80\n\t"
"v_mov_b32 v86, v80\n\t"
"v_mov_b32 v87, v80\n\t"
"v_mov_b32 v88, v80\n\t"
"v_mov_b32 v89, v80\n\t"
"v_mov_b32 v90, v80\n\t"
"v_mov_b32 v91, v80\n\t"
"v_mov_b32 v92, v80\n\t"
"v_mov_b32 v93, v80\n\t"
"v_mov_b32 v94, v80\n\t"
"v_mov_b32 v95, v80\n\t"
"v_mov_b32 v192, 0\n\t"
"v_mov_b32 v193, 0\n\t"
"v_mov_b32 v194, 0\n\t"
"v_mov_b32 v195, 0\n\t"
"v_mov_b32 v196, 0\n\t"
"v_mov_b32 v197, 0\n\t"
"v_mov_b32 v198, 0\n\t"
"v_mov_b32 v199, 0\n\t"
"v_mov_b32 v200, 0\n\t"
"v_mov_b32 v201, 0\n\t"
"v_mov_b32 v202, 0\n\t"
"v_mov_b32 v203, 0\n\t"
"v_mov_b32 v204, 0\n\t"
"v_mov_b32 v205, 0\n\t"
"v_mov_b32 v206, 0\n\t"
"v_mov_b32 v207, 0\n\t"
"v_mov_b32 v208, 0\n\t"
"v_mov_b32 v209, 0\n\t"
"v_mov_b32 v210, 0\n\t"
"v_mov_b32 v211, 0\n\t"
"v_mov_b32 v212, 0\n\t"
"v_mov_b32 v213, 0\n\t"
"v_mov_b32 v214, 0\n\t"
"v_mov_b32 v215, 0\n\t"
"v_mov_b32 v216, 0\n\t"
"v_mov_b32 v217, 0\n\t"
"v_mov_b32 v218, 0\n\t"
"v_mov_b32 v219, 0\n\t"
"v_mov_b32 v220, 0\n\t"
"v_mov_b32 v221, 0\n\t"
"v_mov_b32 v222, 0\n\t"
"v_mov_b32 v223, 0\n\t"
"v_mov_b32 v224, 0\n\t"
"v_mov_b32 v225, 0\n\t"
"v_mov_b32 v226, 0\n\t"
"v_mov_b32 v227, 0\n\t"
"v_mov_b32 v228, 0\n\t"
"v_mov_b32 v229, 0\n\t"
"v_mov_b32 v230, 0\n\t"
"v_mov_b32 v231, 0\n\t"
"s_lshl_b32 s60, s47, 18\n\t"
"s_mov_b32 s56, 64\n\t"
"L_fb2_%=:\n\t"
"buffer_load_dwordx4 v[128:131], v238, s[24:27], s60 offen offset:0 sc1\n\t"
"buffer_load_dwordx4 v[132:135], v238, s[24:27], s60 offen offset:1024 sc1\n\t"
"buffer_load_dwordx4 v[136:139], v238, s[24:27], s60 offen offset:2048 sc1\n\t"
"buffer_load_dwordx4 v[140:143], v238, s[24:27], s60 offen offset:3072 sc1\n\t"
"s_waitcnt vmcnt(0)\n\t"
"v_mfma_f32_32x32x16_bf16 v[32:47], v[128:131], v[96:99], v[64:79]\n\t"
"v_mfma_f32_32x32x16_bf16 v[48:63], v[128:131], v[104:107], v[80:95]\n\t"
"v_mfma_f32_32x32x16_bf16 v[32:47], v[132:135], v[100:103], v[32:47]\n\t"
"v_mfma_f32_32x32x16_bf16 v[48:63], v[132:135], v[108:111], v[48:63]\n\t"
"s_nop 15\n\t"
"v_exp_f32 v32, v32\n\t"
"v_exp_f32 v33, v33\n\t"
"v_exp_f32 v34, v34\n\t"
"v_exp_f32 v35, v35\n\t"
"v_exp_f32 v36, v36\n\t"
"v_exp_f32 v37, v37\n\t"
"v_exp_f32 v38, v38\n\t"
"v_exp_f32 v39, v39\n\t"
"v_exp_f32 v40, v40\n\t"
"v_exp_f32 v41, v41\n\t"
"v_exp_f32 v42, v42\n\t"
"v_exp_f32 v43, v43\n\t"
"v_exp_f32 v44, v44\n\t"
"v_exp_f32 v45, v45\n\t"
"v_exp_f32 v46, v46\n\t"
"v_exp_f32 v47, v47\n\t"
"s_nop 0\n\t"
"v_cvt_pk_bf16_f32 v112, v32, v33\n\t"
"v_cvt_pk_bf16_f32 v113, v34, v35\n\t"
"v_cvt_pk_bf16_f32 v114, v36, v37\n\t"
"v_cvt_pk_bf16_f32 v115, v38, v39\n\t"
"v_cvt_pk_bf16_f32 v116, v40, v41\n\t"
"v_cvt_pk_bf16_f32 v117, v42, v43\n\t"
"v_cvt_pk_bf16_f32 v118, v44, v45\n\t"
"v_cvt_pk_bf16_f32 v119, v46, v47\n\t"
"v_exp_f32 v48, v48\n\t"
"v_exp_f32 v49, v49\n\t"
"v_exp_f32 v50, v50\n\t"
"v_exp_f32 v51, v51\n\t"
"v_exp_f32 v52, v52\n\t"
"v_exp_f32 v53, v53\n\t"
"v_exp_f32 v54, v54\n\t"
"v_exp_f32 v55, v55\n\t"
"v_exp_f32 v56, v56\n\t"
"v_exp_f32 v57, v57\n\t"
"v_exp_f32 v58, v58\n\t"
"v_exp_f32 v59, v59\n\t"
"v_exp_f32 v60, v60\n\t"
"v_exp_f32 v61, v61\n\t"
"v_exp_f32 v62, v62\n\t"
"v_exp_f32 v63, v63\n\t"
"s_nop 0\n\t"
"v_cvt_pk_bf16_f32 v120, v48, v49\n\t"
"v_cvt_pk_bf16_f32 v121, v50, v51\n\t"
"v_cvt_pk_bf16_f32 v122, v52, v53\n\t"
"v_cvt_pk_bf16_f32 v123, v54, v55\n\t"
"v_cvt_pk_bf16_f32 v124, v56, v57\n\t"
"v_cvt_pk_bf16_f32 v125, v58, v59\n\t"
"v_cvt_pk_bf16_f32 v126, v60, v61\n\t"
"v_cvt_pk_bf16_f32 v127, v62, v63\n\t"
"s_nop 1\n\t"
"v_mfma_f32_32x32x16_bf16 v[192:207], v[136:139], v[112:115], v[192:207]\n\t"
"v_mfma_f32_32x32x16_bf16 v[192:207], v[140:143], v[116:119], v[192:207]\n\t"
"v_smfmac_f32_16x16x64_bf16 v[224:227], v[152:155], v[112:119], v158\n\t"
"v_mfma_f32_32x32x16_bf16 v[208:223], v[136:139], v[120:123], v[208:223]\n\t"
"v_mfma_f32_32x32x16_bf16 v[208:223], v[140:143], v[124:127], v[208:223]\n\t"
"v_smfmac_f32_16x16x64_bf16 v[228:231], v[152:155], v[120:127], v158\n\t"
"s_add_u32 s60, s60, 4096\n\t"
"s_sub_u32 s56, s56, 1\n\t"
"s_cmp_lg_u32 s56, 0\n\t"
"s_nop 15\n\t"
"s_cbranch_scc1 L_fb2_%=\n\t"
"v_and_b32 v240, 16, v252\n\t"
"v_cmp_ne_u32 vcc, 0, v240\n\t"
"s_nop 1\n\t"
"v_cndmask_b32 v242, v224, v225, vcc\n\t"
"v_cndmask_b32 v243, v228, v229, vcc\n\t"
"v_cndmask_b32 v224, v226, v227, vcc\n\t"
"v_cndmask_b32 v228, v230, v231, vcc\n\t"
"v_add_f32 v242, v242, v224\n\t"
"v_add_f32 v243, v243, v228\n\t"
"ds_read_b128 v[160:163], v250 offset:32768\n\t"
"ds_read_b128 v[164:167], v250 offset:34816\n\t"
"ds_read_b128 v[168:171], v250 offset:40960\n\t"
"ds_read_b128 v[172:175], v250 offset:43008\n\t"
"ds_read_b128 v[128:131], v248 offset:0\n\t"
"ds_read_b128 v[132:135], v248 offset:2048\n\t"
"ds_read_b128 v[136:139], v248 offset:4096\n\t"
"ds_read_b128 v[140:143], v248 offset:6144\n\t"
"ds_read_b128 v[144:147], v248 offset:16384\n\t"
"ds_read_b128 v[148:151], v248 offset:18432\n\t"
"ds_read_b128 v[152:155], v248 offset:20480\n\t"
"ds_read_b128 v[156:159], v248 offset:22528\n\t"
"s_branch L_att_ok_%=\n\t"
"L_att_pub_%=:\n\t"
"s_waitcnt vmcnt(0)\n\t"
"v_mov_b32 v240, s53\n\t"
"v_mov_b32 v241, 0\n\t"
"s_mov_b64 exec, 1\n\t"
"buffer_store_dword v240, v241, s[24:27], s65 offen sc1\n\t"
"s_mov_b64 exec, -1\n\t"
"s_branch L_att_pub_r_%=\n\t"
"L_att_g_%=:\n\t"
"v_and_b32 v240, 31, v252\n\t"
"v_lshlrev_b32 v240, 2, v240\n\t"
"v_lshlrev_b32 v241, 2, v253\n\t"
"buffer_load_dword v236, v240, s[24:27], s66 offen sc1\n\t"
"buffer_load_dword v237, v240, s[24:27], s66 offen offset:128 sc1\n\t"
"buffer_load_dword v244, v241, s[24:27], s77 offen offset:0 sc1\n\t"
"buffer_load_dword v245, v241, s[24:27], s77 offen offset:64 sc1\n\t"
"buffer_load_dword v246, v241, s[24:27], s77 offen offset:128 sc1\n\t"
"buffer_load_dword v247, v241, s[24:27], s77 offen offset:192 sc1\n\t"
"s_branch L_att_g_r_%=\n\t"
"L_xr0_%=:\n\t"
"s_sub_u32 s78, s78, 1\n\t"
"s_cmp_gt_i32 s78, 0\n\t"
"s_cbranch_scc0 L_xs0_%=\n\t"
"buffer_load_dwordx4 v[80:83], v227, s[24:27], s91 offen offset:0 sc1\n\t"
"buffer_load_dwordx4 v[64:67], v227, s[24:27], s87 offen offset:0 sc1\n\t"
"buffer_load_dwordx4 v[96:99], v227, s[24:27], s92 offen offset:0 sc1\n\t"
"s_waitcnt vmcnt(0)\n\t"
"s_branch L_xc0_%=\n\t"
"L_xr1_%=:\n\t"
"s_sub_u32 s78, s78, 1\n\t"
"s_cmp_gt_i32 s78, 0\n\t"
"s_cbranch_scc0 L_xs1_%=\n\t"
"buffer_load_dwordx4 v[84:87], v227, s[24:27], s91 offen offset:1024 sc1\n\t"
"buffer_load_dwordx4 v[68:71], v227, s[24:27], s87 offen offset:1024 sc1\n\t"
"buffer_load_dwordx4 v[100:103], v227, s[24:27], s92 offen offset:1024 sc1\n\t"
"s_waitcnt vmcnt(0)\n\t"
"s_branch L_xc1_%=\n\t"
"L_xr2_%=:\n\t"
"s_sub_u32 s78, s78, 1\n\t"
"s_cmp_gt_i32 s78, 0\n\t"
"s_cbranch_scc0 L_xs2_%=\n\t"
"buffer_load_dwordx4 v[88:91], v227, s[24:27], s91 offen offset:2048 sc1\n\t"
"buffer_load_dwordx4 v[72:75], v227, s[24:27], s87 offen offset:2048 sc1\n\t"
"buffer_load_dwordx4 v[104:107], v227, s[24:27], s92 offen offset:2048 sc1\n\t"
"s_waitcnt vmcnt(0)\n\t"
"s_branch L_xc2_%=\n\t"
"L_xr3_%=:\n\t"
"s_sub_u32 s78, s78, 1\n\t"
"s_cmp_gt_i32 s78, 0\n\t"
"s_cbranch_scc0 L_xs3_%=\n\t"
"buffer_load_dwordx4 v[92:95], v227, s[24:27], s91 offen offset:3072 sc1\n\t"
"buffer_load_dwordx4 v[76:79], v227, s[24:27], s87 offen offset:3072 sc1\n\t"
"buffer_load_dwordx4 v[108:111], v227, s[24:27], s92 offen offset:3072 sc1\n\t"
"s_waitcnt vmcnt(0)\n\t"
"s_branch L_xc3_%=\n\t"
"L_end_%=:\n\t"

        :
        : "s"(__builtin_amdgcn_kernarg_segment_ptr()), "s"(blockIdx.x), "v"(threadIdx.x), "s"((unsigned)(size_t)lds)
        : "v0","v1","v2","v3","v4","v5","v6","v7","v8","v9","v10","v11","v12","v13","v14","v15","v16","v17","v18","v19","v20","v21","v22","v23","v24","v25","v26","v27","v28","v29","v30","v31","v32","v33","v34","v35","v36","v37","v38","v39","v40","v41","v42","v43","v44","v45","v46","v47","v48","v49","v50","v51","v52","v53","v54","v55","v56","v57","v58","v59","v60","v61","v62","v63","v64","v65","v66","v67","v68","v69","v70","v71","v72","v73","v74","v75","v76","v77","v78","v79","v80","v81","v82","v83","v84","v85","v86","v87","v88","v89","v90","v91","v92","v93","v94","v95","v96","v97","v98","v99","v100","v101","v102","v103","v104","v105","v106","v107","v108","v109","v110","v111","v112","v113","v114","v115","v116","v117","v118","v119","v120","v121","v122","v123","v124","v125","v126","v127","v128","v129","v130","v131","v132","v133","v134","v135","v136","v137","v138","v139","v140","v141","v142","v143","v144","v145","v146","v147","v148","v149","v150","v151","v152","v153","v154","v155","v156","v157","v158","v159","v160","v161","v162","v163","v164","v165","v166","v167","v168","v169","v170","v171","v172","v173","v174","v175","v176","v177","v178","v179","v180","v181","v182","v183","v184","v185","v186","v187","v188","v189","v190","v191","v192","v193","v194","v195","v196","v197","v198","v199","v200","v201","v202","v203","v204","v205","v206","v207","v208","v209","v210","v211","v212","v213","v214","v215","v216","v217","v218","v219","v220","v221","v222","v223","v224","v225","v226","v227","v228","v229","v230","v231","v232","v233","v234","v235","v236","v237","v238","v239","v240","v241","v242","v243","v244","v245","v246","v247","v248","v249","v250","v251","v252","v253","v254","s4","s5","s6","s7","s8","s9","s10","s11","s12","s13","s14","s15","s16","s17","s18","s19","s20","s21","s22","s23","s24","s25","s26","s27","s28","s29","s30","s31","s33","s34","s35","s36","s37","s38","s39","s40","s41","s42","s43","s44","s45","s46","s47","s48","s49","s50","s51","s52","s53","s54","s55","s56","s57","s58","s59","s60","s61","s62","s63","s64","s65","s66","s67","s68","s69","s70","s71","s72","s73","s74","s75","s76","s77","s78","s79","s80","s81","s82","s83","s84","s85","s86","s87","s88","s89","s90","s91","s92","s93","s94","s95","s96","s97","s98","s99","vcc","scc","memory");
}

extern "C" void kernel_launch(void* const* d_in, const int* in_sizes, int n_in,
                              void* d_out, int out_size, void* d_ws, size_t ws_size,
                              hipStream_t stream) {
    fused_kernel<<<256, 512, 0, stream>>>(d_in[0], d_in[1], d_in[2], d_in[3], d_in[4], d_in[6], d_in[7], d_in[8], d_out, d_ws);
}
```
